```python
import torch

def _softmax(x, axis=-1):
    return torch.softmax(x, dim=axis)
def _gelu(x):
    return torch.nn.functional.gelu(x, approximate="tanh")
def _silu(x):
    return torch.nn.functional.silu(x)
def _sigmoid(x):
    return torch.sigmoid(x)
def _layernorm(x, axis=-1, eps=1e-5):
    m = x.mean(dim=axis, keepdim=True)
    v = x.var(dim=axis, keepdim=True, unbiased=False)
    return (x - m) / torch.sqrt(v + eps)
def _scatter_add(tgt, idx, src, axis=0):
    return tgt.clone().index_add_(axis, idx, src)
def _segment_sum(src, seg, n):
    out = torch.zeros((n,) + src.shape[1:], dtype=src.dtype)
    return out.index_add_(0, seg, src)
def _pad(x, pw):
    flat = tuple(v for pair in reversed(pw) for v in pair)
    return torch.nn.functional.pad(x, flat)

kernel_name = 'sequential_preLN__sliding_block_cached__dense_activation_gelu__decode'

def setup_inputs():

    _w = (lambda a: (lambda t: t.to(torch.bfloat16) if t.is_floating_point() else t)(torch.as_tensor(a)))
    def _A_setup():
        B, Sq, D = 2, 1, 512
        Hq, Hkv, Sk, Dh = 32, 8, 8192, 128
        assert Hq % Hkv == 0, (Hq, Hkv)
        return {
            "x": _w((lambda *s: torch.randn(*s, dtype=torch.bfloat16) * 0.05)(B, Sq, D)),
            "k_cache": _w((lambda *s: torch.randn(*s, dtype=torch.bfloat16) * 3.2)(B, Hkv, Sk, Dh)),
            "v_cache": _w((lambda *s: torch.randn(*s, dtype=torch.bfloat16) * 0.8)(B, Hkv, Sk, Dh)),
            "wq": _w((lambda *s: torch.randn(*s, dtype=torch.bfloat16) * 3.2)(D, Hq * Dh) * (D ** -0.5)),
            "wo": _w((lambda *s: torch.randn(*s, dtype=torch.bfloat16) * 0.8)(Hq * Dh, D) * ((Hq * Dh) ** -0.5)),
        }
    _A_in = _A_setup()

    def _B_setup():
        B, S, D, F = 2, 1, 512, 1792
        return {
            "x": _w((lambda *s: torch.randn(*s, dtype=torch.bfloat16) * 0.05)(B, S, D)),
            "w1": _w((lambda *s: torch.randn(*s, dtype=torch.bfloat16) * 3.2)(D, F) * (D ** -0.5)),
            "b1": _w((lambda *s: torch.randn(*s, dtype=torch.bfloat16) * 3.2)(F) * 0.01),
            "w2": _w((lambda *s: torch.randn(*s, dtype=torch.bfloat16) * 0.4)(F, D) * (F ** -0.5)),
            "b2": _w((lambda *s: torch.randn(*s, dtype=torch.bfloat16) * 0.4)(D) * 0.01),
        }
    _B_in = _B_setup()

    return {
        "A_x": _A_in["x"],
        "A_k_cache": _A_in["k_cache"],
        "A_v_cache": _A_in["v_cache"],
        "A_wq": _A_in["wq"],
        "A_wo": _A_in["wo"],
        "B_x": _B_in["x"],
        "B_w1": _B_in["w1"],
        "B_b1": _B_in["b1"],
        "B_w2": _B_in["w2"],
        "B_b2": _B_in["b2"],
    }

def reference(A_x, A_k_cache, A_v_cache, A_wq, A_wo, B_x, B_w1, B_b1, B_w2, B_b2):
    def _A_ref(x, k_cache, v_cache, wq, wo):
        B, Sq, D = x.shape
        Hq, Dh = 32, 128
        Hkv, Sk = k_cache.shape[1], k_cache.shape[2]
        W = min(4096, max(256, Sk // 4))
        rep = Hq // Hkv
        q = (lambda x, *d: x.permute(*d))((x @ wq).reshape(B, Sq, Hq, Dh), 0, 2, 1, 3)
        k = (lambda x, n, axis: torch.repeat_interleave(x, n, dim=axis))(k_cache[:, :, Sk - W:, :], rep, axis=1)
        v = (lambda x, n, axis: torch.repeat_interleave(x, n, dim=axis))(v_cache[:, :, Sk - W:, :], rep, axis=1)
        scores = torch.einsum("bhqd,bhkd->bhqk", q, k) * (Dh ** -0.5)
        p = _softmax(scores, axis=-1)
        o = torch.einsum("bhqk,bhkd->bhqd", p, v)
        return (lambda x, *d: x.permute(*d))(o, 0, 2, 1, 3).reshape(B, Sq, Hq * Dh) @ wo

    def _B_ref(x, w1, b1, w2, b2):
        h = torch.einsum("bsd,df->bsf", x, w1) + b1
        h = _gelu(h)
        return torch.einsum("bsf,fd->bsd", h, w2) + b2

    x = A_x
    h = x + _A_ref(_layernorm(x, axis=-1), A_k_cache, A_v_cache, A_wq, A_wo)
    h2 = h + _B_ref(_layernorm(h, axis=-1), B_w1, B_b1, B_w2, B_b2)
    return h2
```

```cpp
#include <hip/hip_runtime.h>
#include <hip/hip_bf16.h>
#include <stdint.h>

typedef unsigned short u16;
__device__ __forceinline__ float bf2f(u16 b) { return __uint_as_float(((unsigned)b) << 16); }
__device__ __forceinline__ float rbf(float f) {
  __hip_bfloat16 h = __float2bfloat16(f); return __bfloat162float(h);
}
#define MIMIC 1
__device__ __forceinline__ float R(float f) { return MIMIC ? rbf(f) : f; }

constexpr int B = 2, D = 512, HQ = 32, HKV = 8, SK = 8192, DH = 128, W = 2048, F = 1792;
constexpr int NSPLIT = 16, KPS = W / NSPLIT;

__device__ float block_sum(float v, float* red) {
  for (int o = 32; o > 0; o >>= 1) v += __shfl_xor(v, o);
  int w = threadIdx.x >> 6, nw = blockDim.x >> 6;
  __syncthreads();
  if ((threadIdx.x & 63) == 0) red[w] = v;
  __syncthreads();
  float s = 0; for (int i = 0; i < nw; ++i) s += red[i];
  return s;
}

__device__ void ln512(const float* v, float* out, float* red) {
  int t = threadIdx.x;
  float a = v[t], b = v[t + 256];
  float mean = block_sum(a + b, red) * (1.f / 512.f);
  float da = a - mean, db = b - mean;
  float var = block_sum(da * da + db * db, red) * (1.f / 512.f);
  float m = R(mean), vv = R(var);
  float den = R(sqrtf(R(vv + 1e-5f)));
  out[t] = R(R(a - m) / den);
  out[t + 256] = R(R(b - m) / den);
  __syncthreads();
}

__global__ void __launch_bounds__(256) k_q(const u16* x, const u16* wq, float* q) {
  __shared__ float xs[2][512], xn[2][512], red[8], part[4][2][64];
  int t = threadIdx.x;
  for (int b = 0; b < 2; ++b) { xs[b][t] = bf2f(x[b * 512 + t]); xs[b][t + 256] = bf2f(x[b * 512 + t + 256]); }
  __syncthreads();
  ln512(xs[0], xn[0], red); ln512(xs[1], xn[1], red);
  int c = blockIdx.x * 64 + (t & 63), kg = t >> 6;
  float a0 = 0, a1 = 0;
  for (int k = kg * 128; k < kg * 128 + 128; ++k) { float w = bf2f(wq[(size_t)k * 4096 + c]); a0 += xn[0][k] * w; a1 += xn[1][k] * w; }
  part[kg][0][t & 63] = a0; part[kg][1][t & 63] = a1;
  __syncthreads();
  if (t < 128) { int b = t >> 6, cc = t & 63; float s = part[0][b][cc] + part[1][b][cc] + part[2][b][cc] + part[3][b][cc]; q[b * 4096 + blockIdx.x * 64 + cc] = R(s); }
}

__global__ void __launch_bounds__(256) k_attn(const float* q, const u16* kc, const u16* vc, float* pm, float* pl, float* po) {
  __shared__ float qs[4][128], sc[4][KPS], red[8];
  int s = blockIdx.x, kvh = blockIdx.y, b = blockIdx.z, t = threadIdx.x;
  for (int i = t; i < 512; i += 256) qs[i >> 7][i & 127] = q[b * 4096 + kvh * 512 + i];
  __syncthreads();
  const u16* kb = kc + ((size_t)(b * HKV + kvh) * SK + (SK - W) + s * KPS) * DH;
  const u16* vb = vc + ((size_t)(b * HKV + kvh) * SK + (SK - W) + s * KPS) * DH;
  for (int i = t; i < 4 * KPS; i += 256) {
    int h = i / KPS, key = i % KPS; float a = 0;
    for (int d = 0; d < 128; ++d) a += qs[h][d] * bf2f(kb[key * DH + d]);
    sc[h][key] = R(R(a) * R(0.08838834764831845f));
  }
  __syncthreads();
  int h = t >> 6, lane = t & 63;
  float m = fmaxf(sc[h][lane], sc[h][lane + 64]);
  for (int o = 32; o > 0; o >>= 1) m = fmaxf(m, __shfl_xor(m, o));
  float p0 = __expf(sc[h][lane] - m), p1 = __expf(sc[h][lane + 64] - m);
  float l = p0 + p1;
  for (int o = 32; o > 0; o >>= 1) l += __shfl_xor(l, o);
  sc[h][lane] = p0; sc[h][lane + 64] = p1;
  __syncthreads();
  float o0 = 0, o1 = 0;
  for (int key = 0; key < KPS; ++key) { float p = sc[h][key]; o0 += p * bf2f(vb[key * DH + lane]); o1 += p * bf2f(vb[key * DH + lane + 64]); }
  int row = (b * HQ + kvh * 4 + h) * NSPLIT + s;
  po[row * 128 + lane] = o0; po[row * 128 + lane + 64] = o1;
  if (lane == 0) { pm[row] = m; pl[row] = l; }
}

__global__ void __launch_bounds__(256) k_wo(const float* pm, const float* pl, const float* po, const u16* wo, float* y) {
  __shared__ float o[2][128];
  int h = blockIdx.x, t = threadIdx.x;
  { int b = t >> 7, d = t & 127; int row0 = (b * HQ + h) * NSPLIT;
    float M = -INFINITY; for (int s = 0; s < NSPLIT; ++s) M = fmaxf(M, pm[row0 + s]);
    float L = 0, a = 0; for (int s = 0; s < NSPLIT; ++s) { float w = __expf(pm[row0 + s] - M); L += w * pl[row0 + s]; a += w * po[(row0 + s) * 128 + d]; }
    o[b][d] = R(a / L); }
  __syncthreads();
  for (int c = t; c < 512; c += 256) {
    float a0 = 0, a1 = 0;
    for (int d = 0; d < 128; ++d) { float w = bf2f(wo[(size_t)(h * 128 + d) * 512 + c]); a0 += o[0][d] * w; a1 += o[1][d] * w; }
    y[(h * 2 + 0) * 512 + c] = a0; y[(h * 2 + 1) * 512 + c] = a1;
  }
}

__global__ void __launch_bounds__(256) k_mlp(const u16* x, const float* y, const u16* w1, const u16* b1, const u16* w2, float* hout, float* z) {
  __shared__ float hs[2][512], hn[2][512], red[8], part[4][2][64], g[2][64];
  int t = threadIdx.x;
  for (int i = t; i < 1024; i += 256) { int b = i >> 9, c = i & 511; float s = 0; for (int h = 0; h < 32; ++h) s += y[(h * 2 + b) * 512 + c]; hs[b][c] = R(bf2f(x[i]) + R(s)); }
  __syncthreads();
  if (blockIdx.x == 0) for (int i = t; i < 1024; i += 256) hout[i] = hs[i >> 9][i & 511];
  ln512(hs[0], hn[0], red); ln512(hs[1], hn[1], red);
  int f = blockIdx.x * 64 + (t & 63), kg = t >> 6;
  float a0 = 0, a1 = 0;
  for (int k = kg * 128; k < kg * 128 + 128; ++k) { float w = bf2f(w1[(size_t)k * F + f]); a0 += hn[0][k] * w; a1 += hn[1][k] * w; }
  part[kg][0][t & 63] = a0; part[kg][1][t & 63] = a1;
  __syncthreads();
  if (t < 128) { int b = t >> 6, cc = t & 63; float s = part[0][b][cc] + part[1][b][cc] + part[2][b][cc] + part[3][b][cc];
    float u = R(R(s) + bf2f(b1[blockIdx.x * 64 + cc]));
    float inner = 0.7978845608028654f * (u + 0.044715f * u * u * u);
    g[b][cc] = R(0.5f * u * (1.f + tanhf(inner))); }
  __syncthreads();
  for (int c = t; c < 512; c += 256) {
    float s0 = 0, s1 = 0;
    for (int j = 0; j < 64; ++j) { float w = bf2f(w2[(size_t)(blockIdx.x * 64 + j) * 512 + c]); s0 += g[0][j] * w; s1 += g[1][j] * w; }
    z[(blockIdx.x * 2 + 0) * 512 + c] = s0; z[(blockIdx.x * 2 + 1) * 512 + c] = s1;
  }
}

__global__ void __launch_bounds__(256) k_out(const float* hout, const float* z, const u16* b2, __hip_bfloat16* out) {
  int i = blockIdx.x * 256 + threadIdx.x; int b = i >> 9, c = i & 511;
  float s = 0; for (int j = 0; j < 28; ++j) s += z[(j * 2 + b) * 512 + c];
  out[i] = __float2bfloat16(hout[i] + R(R(s) + bf2f(b2[c])));
}

extern "C" void kernel_launch(void* const* d_in, const int* in_sizes, int n_in, void* d_out, int out_size, void* d_ws, size_t ws_size, hipStream_t stream) {
  const u16* x = (const u16*)d_in[0]; const u16* kc = (const u16*)d_in[1]; const u16* vc = (const u16*)d_in[2];
  const u16* wq = (const u16*)d_in[3]; const u16* wo = (const u16*)d_in[4];
  const u16* w1 = (const u16*)d_in[6]; const u16* b1 = (const u16*)d_in[7]; const u16* w2 = (const u16*)d_in[8]; const u16* b2 = (const u16*)d_in[9];
  float* ws = (float*)d_ws;
  float* q = ws; float* pm = q + 8192; float* pl = pm + 1024; float* po = pl + 1024; float* y = po + 1024 * 128; float* hout = y + 64 * 512; float* z = hout + 1024;
  k_q<<<64, 256, 0, stream>>>(x, wq, q);
  k_attn<<<dim3(NSPLIT, HKV, B), 256, 0, stream>>>(q, kc, vc, pm, pl, po);
  k_wo<<<32, 256, 0, stream>>>(pm, pl, po, wo, y);
  k_mlp<<<28, 256, 0, stream>>>(x, y, w1, b1, w2, hout, z);
  k_out<<<4, 256, 0, stream>>>(hout, z, b2, (__hip_bfloat16*)d_out);
}
```

```cpp
#include <hip/hip_runtime.h>
#include <hip/hip_bf16.h>
#include <stdint.h>

#define STAMPS 0
#define LATE_W 1

typedef unsigned short u16;
typedef unsigned v4u __attribute__((ext_vector_type(4)));
typedef unsigned v2u __attribute__((ext_vector_type(2)));
typedef float f32x4 __attribute__((ext_vector_type(4)));
typedef short s16x8 __attribute__((ext_vector_type(8)));
typedef short s16x4 __attribute__((ext_vector_type(4)));
typedef __bf16 b16x8 __attribute__((ext_vector_type(8)));
#define LAS __attribute__((address_space(3)))

constexpr int NWG = 256, NT = 576;
constexpr int NM = 112;
constexpr int DM = 512, FF = 1792, SK = 8192, WIN = 2048;
constexpr int WS_E = 0, WS_QX = 4096, WS_ML = WS_QX + 32768, WS_HX = WS_ML + 32768, WS_PB = 131072;
constexpr int WS_PC = WS_PB + (1 << 20), WS_PD = WS_PC + 64 * 64 * 128, WS_END = WS_PD + 64 * NM * 128;
constexpr int WS_DBG = 8 << 20;
constexpr int L_V = 0, L_WQ = 32768, L_WO = 49152, L_W1 = 65536, L_W2 = 81920, L_XN = 98304, L_Q = L_XN + 2048, L_P = L_Q + 1024,
              L_MAX = L_P + 1024, L_SUM = L_MAX + 128, L_O = L_SUM + 128, L_G = L_O + 256, L_T = L_G + 64, L_TAG = L_T + 64, L_END = L_TAG + 16;
constexpr int OOB = 0x7ffffff0;
constexpr int SPIN_CAP = 1 << 16;

struct Params { const u16 *x, *kc, *vc, *wq, *wo, *w1, *b1, *w2, *b2; u16* out; unsigned char* ws; };

__device__ __forceinline__ float bf2f(unsigned b) { return __builtin_bit_cast(float, b << 16); }
__device__ __forceinline__ float bflo(unsigned w) { return __builtin_bit_cast(float, w << 16); }
__device__ __forceinline__ float bfhi(unsigned w) { return __builtin_bit_cast(float, w & 0xffff0000u); }
__device__ __forceinline__ unsigned pk2(float lo, float hi) {
  typedef __bf16 b2 __attribute__((ext_vector_type(2)));
  typedef float f2 __attribute__((ext_vector_type(2)));
  f2 v = {lo, hi};
  return __builtin_bit_cast(unsigned, __builtin_convertvector(v, b2));
}
__device__ __forceinline__ float rbf(float f) { return bflo(pk2(f, f)); }
template <int CTRL> __device__ __forceinline__ float dpp(float x) {
  return __builtin_bit_cast(float, __builtin_amdgcn_update_dpp(0, __builtin_bit_cast(int, x), CTRL, 0xf, 0xf, true));
}
template <int CTRL> __device__ __forceinline__ unsigned dppu(unsigned x) { return (unsigned)__builtin_amdgcn_update_dpp(0, (int)x, CTRL, 0xf, 0xf, true); }
__device__ __forceinline__ float row_max(float v) { v = fmaxf(v, dpp<0x128>(v)); v = fmaxf(v, dpp<0x124>(v)); v = fmaxf(v, dpp<0x122>(v)); v = fmaxf(v, dpp<0x121>(v)); return v; }
__device__ __forceinline__ float row_sum(float v) { v += dpp<0x128>(v); v += dpp<0x124>(v); v += dpp<0x122>(v); v += dpp<0x121>(v); return v; }
__device__ __forceinline__ float xrow_sum(float x) {
  auto s = __builtin_amdgcn_permlane16_swap(__float_as_uint(x), __float_as_uint(x), false, false);
  x = __uint_as_float(s[0]) + __uint_as_float(s[1]);
  auto t = __builtin_amdgcn_permlane32_swap(__float_as_uint(x), __float_as_uint(x), false, false);
  return __uint_as_float(t[0]) + __uint_as_float(t[1]);
}
__device__ __forceinline__ float wave_sum(float v) { return xrow_sum(row_sum(v)); }
__device__ __forceinline__ f32x4 mfma32(v4u a, v4u b, f32x4 c) {
  return __builtin_amdgcn_mfma_f32_16x16x32_bf16(__builtin_bit_cast(b16x8, a), __builtin_bit_cast(b16x8, b), c, 0, 0, 0);
}
__device__ __forceinline__ f32x4 mfma16(v2u a, v2u b, f32x4 c) {
  return __builtin_amdgcn_mfma_f32_16x16x16bf16_1k(__builtin_bit_cast(s16x4, a), __builtin_bit_cast(s16x4, b), c, 0, 0, 0);
}
__device__ __forceinline__ v2u tr_read(LAS unsigned char* L, int off) {
  return __builtin_bit_cast(v2u, __builtin_amdgcn_ds_read_tr16_b64_v4i16((LAS s16x4*)(L + off)));
}
__device__ __forceinline__ v4u lds128(LAS unsigned char* L, int off) { return *(LAS v4u*)(L + off); }
__device__ __forceinline__ v4u ldg_nt(const void* base, int voff) {
  v4u r; asm volatile("global_load_dwordx4 %0, %1, %2 nt" : "=v"(r) : "v"(voff), "s"(base) : "memory"); return r;
}
__device__ __forceinline__ v4u ldg(const void* base, int voff) {
  v4u r; asm volatile("global_load_dwordx4 %0, %1, %2" : "=v"(r) : "v"(voff), "s"(base) : "memory"); return r;
}
__device__ __forceinline__ void dma16(const void* base, int voff, unsigned lds_dst) {
  unsigned keep;
  asm volatile("s_mov_b32 %0, m0\n\ts_mov_b32 m0, %3\n\ts_nop 0\n\tglobal_load_lds_dwordx4 %1, %2 nt\n\ts_mov_b32 m0, %0"
               : "=&s"(keep) : "v"(voff), "s"(base), "s"(lds_dst) : "memory");
}
#define WAIT_VM(n) asm volatile("s_waitcnt vmcnt(" #n ")" ::: "memory")
#define BAR() asm volatile("s_waitcnt lgkmcnt(0)\n\ts_barrier" ::: "memory")
__device__ __forceinline__ int swz(int row) { return ((row & 3) << 2) | ((row >> 2) & 3); }

__device__ __forceinline__ void put(__amdgpu_buffer_rsrc_t rs, int off, unsigned a, unsigned b, unsigned tag) {
  v4u v = {a, tag, b, tag};
  __builtin_amdgcn_raw_buffer_store_b128(v, rs, off, 0, 16);
}
__device__ __forceinline__ unsigned fu(float f) { return __builtin_bit_cast(unsigned, f); }
__device__ __forceinline__ float uf(unsigned u) { return __builtin_bit_cast(float, u); }

template <int N> __device__ __forceinline__ void gather(__amdgpu_buffer_rsrc_t rs, const int (&off)[N], unsigned tag, unsigned (&a)[N], unsigned (&b)[N]) {
  int o[N];
#pragma unroll
  for (int k = 0; k < N; ++k) { o[k] = off[k]; a[k] = 0; b[k] = 0; }
  for (int spin = 0; spin < SPIN_CAP; ++spin) {
    v4u n[N];
#pragma unroll
    for (int k = 0; k < N; ++k) n[k] = __builtin_amdgcn_raw_buffer_load_b128(rs, o[k], 0, 16);
    __builtin_amdgcn_sched_barrier(0);
    bool all = true;
#pragma unroll
    for (int k = 0; k < N; ++k) {
      const bool pend = o[k] != OOB;
      const bool m = pend && (n[k].y == tag) && (n[k].w == tag);
      a[k] = m ? n[k].x : a[k]; b[k] = m ? n[k].z : b[k];
      o[k] = m ? OOB : o[k];
      all = all && (m || !pend);
    }
    if (__all(all)) break;
    asm volatile("" ::: "memory");
  }
}

__device__ __forceinline__ void ln2(float (&v)[2][8]) {
#pragma unroll
  for (int b = 0; b < 2; ++b) {
    float s = 0.f;
#pragma unroll
    for (int i = 0; i < 8; ++i) s += v[b][i];
    const float mean = wave_sum(s) * (1.f / DM);
    float q = 0.f;
#pragma unroll
    for (int i = 0; i < 8; ++i) { const float d = v[b][i] - mean; q = fmaf(d, d, q); }
    const float var = wave_sum(q) * (1.f / DM);
    const float mb = rbf(mean);
    const float den = rbf(sqrtf(rbf(rbf(var) + 1e-5f)));
#pragma unroll
    for (int i = 0; i < 8; ++i) v[b][i] = rbf(v[b][i] - mb) / den;
  }
}

__device__ __forceinline__ f32x4 gemv16(LAS unsigned char* L, int tile, int lane) {
  const int q = lane >> 4, m = lane & 15;
  const int aoff = L_XN + (m & 1) * 1024 + q * 16;
  const int boff = tile + (8 * q + (m >> 2)) * 32 + (m & 3) * 8;
  f32x4 acc0 = {0.f, 0.f, 0.f, 0.f}, acc1 = {0.f, 0.f, 0.f, 0.f};
#pragma unroll
  for (int i = 0; i < 16; i += 2) {
    const v4u a0 = lds128(L, aoff + i * 64), a1 = lds128(L, aoff + i * 64 + 64);
    const v2u b00 = tr_read(L, boff + i * 1024), b01 = tr_read(L, boff + i * 1024 + 128);
    const v2u b10 = tr_read(L, boff + i * 1024 + 1024), b11 = tr_read(L, boff + i * 1024 + 1024 + 128);
    acc0 = mfma32(a0, v4u{b00.x, b00.y, b01.x, b01.y}, acc0);
    acc1 = mfma32(a1, v4u{b10.x, b10.y, b11.x, b11.y}, acc1);
  }
  return acc0 + acc1;
}

#define STAMP(i) do { } while (0)

__global__ void __launch_bounds__(NT) fused_block(Params p) {
  extern __shared__ __attribute__((aligned(16))) unsigned char smem[];
  LAS unsigned char* L = (LAS unsigned char*)smem;
  const unsigned lbase = (unsigned)(size_t)L;
  const int tid = threadIdx.x, lane = tid & 63, wave = __builtin_amdgcn_readfirstlane(tid >> 6);
  const int bid = blockIdx.x, g = bid & 7, j = bid >> 3, b = j >> 4, s = j & 15;
  const int q = lane >> 4, n = lane & 15;
  const int hp = j >> 4, dq = (j >> 2) & 3, cb = j & 3;
  const bool is_mlp = j < 14, is_red = (j >= 16) && (j < 24);
  const int mi = g * 14 + j, ri = g * 8 + (j - 16);
  const __amdgpu_buffer_rsrc_t rs = __builtin_amdgcn_make_buffer_rsrc((void*)p.ws, 0, WS_END, 0x00020000);
  STAMP(0);

  if (wave < 8) {
    const size_t kvoff = ((size_t)(b * 8 + g) * SK + (SK - WIN) + s * 128) * 256;
    const unsigned char* kb = (const unsigned char*)p.kc + kvoff + wave * 4096;
    const unsigned char* vb = (const unsigned char*)p.vc + kvoff + wave * 4096;
    v4u kf[4];
#pragma unroll
    for (int i = 0; i < 4; ++i) kf[i] = ldg_nt(kb, n * 256 + q * 16 + i * 64);
#pragma unroll
    for (int i = 0; i < 4; ++i) { const int r = 4 * i + q; dma16(vb, r * 256 + ((n ^ swz(r)) << 4), lbase + L_V + wave * 4096 + i * 1024); }
    auto load_w = [&]() {
#pragma unroll
      for (int i = 0; i < 2; ++i) {
        const int k = 8 * wave + 4 * i + q;
        const int grow = (g * 4 + 2 * hp + (k >> 5)) * 128 + 32 * dq + (k & 31);
        dma16(p.wo, grow * 1024 + cb * 256 + ((n ^ swz(k)) << 4), lbase + L_WO + wave * 2048 + i * 1024);
      }
      if (is_mlp) {
#pragma unroll
        for (int i = 0; i < 2; ++i) {
          const int row = 64 * wave + 32 * i + (lane >> 1);
          dma16(p.w1, row * (FF * 2) + mi * 32 + (lane & 1) * 16, lbase + L_W1 + wave * 2048 + i * 1024);
        }
#pragma unroll
        for (int i = 0; i < 2; ++i) {
          const int row = 2 * wave + i;
          dma16(p.w2, (mi * 16 + row) * 1024 + ((lane ^ ((row & 7) << 1)) << 4), lbase + L_W2 + row * 1024);
        }
      }
    };
    BAR();
    STAMP(1);
    load_w();
    const unsigned tag = *(LAS unsigned*)(L + L_TAG);
    v4u qa[4];
#pragma unroll
    for (int i = 0; i < 4; ++i) qa[i] = lds128(L, L_Q + (n >> 2) * 256 + i * 64 + q * 16);
    if (is_mlp) asm volatile("s_waitcnt vmcnt(10)" : "+v"(kf[0]), "+v"(kf[1]), "+v"(kf[2]), "+v"(kf[3])::"memory");
    else asm volatile("s_waitcnt vmcnt(6)" : "+v"(kf[0]), "+v"(kf[1]), "+v"(kf[2]), "+v"(kf[3])::"memory");
    STAMP(2);
    f32x4 acc = {0.f, 0.f, 0.f, 0.f};
#pragma unroll
    for (int i = 0; i < 4; ++i) acc = mfma32(qa[i], kf[i], acc);
    const float sc = rbf(rbf(acc[0]) * 0.08838834764831845f);
    const float wm = row_max(sc);
    if (n == 0) *(LAS float*)(L + L_MAX + q * 32 + wave * 4) = wm;
    BAR();
    const f32x4 m0 = *(LAS f32x4*)(L + L_MAX + q * 32), m1 = *(LAS f32x4*)(L + L_MAX + q * 32 + 16);
    const float M = fmaxf(fmaxf(fmaxf(m0[0], m0[1]), fmaxf(m0[2], m0[3])), fmaxf(fmaxf(m1[0], m1[1]), fmaxf(m1[2], m1[3])));
    const float pr = __builtin_amdgcn_exp2f((sc - M) * 1.4426950408889634f);
    *(LAS u16*)(L + L_P + q * 256 + (16 * wave + n) * 2) = (u16)pk2(pr, pr);
    const float ws_ = row_sum(pr);
    if (n == 0) *(LAS float*)(L + L_SUM + q * 32 + wave * 4) = ws_;
    if (is_mlp) WAIT_VM(6); else WAIT_VM(2);
    STAMP(3);
    BAR();
    {
      v4u pa[4]; v2u vf[4][2];
#pragma unroll
      for (int i = 0; i < 4; ++i) {
        pa[i] = lds128(L, L_P + (n & 3) * 256 + i * 64 + q * 16);
#pragma unroll
        for (int t = 0; t < 2; ++t) {
          const int row = 32 * i + 8 * q + 4 * t + (n >> 2);
          vf[i][t] = tr_read(L, L_V + row * 256 + (((2 * wave + ((n >> 1) & 1)) ^ swz(row)) << 4) + (n & 1) * 8);
        }
      }
      f32x4 o = {0.f, 0.f, 0.f, 0.f};
#pragma unroll
      for (int i = 0; i < 4; ++i) o = mfma32(pa[i], v4u{vf[i][0].x, vf[i][0].y, vf[i][1].x, vf[i][1].y}, o);
      const float va = q == 0 ? o[0] : o[2], vb2 = q == 0 ? o[1] : o[3];
      const int off = WS_PB + ((((g * 2 + q) * 2 + b) * 16 + s) * 128 + 16 * wave + n) * 16;
      put(rs, q < 2 ? off : OOB, fu(va), fu(vb2), tag);
    }
    STAMP(4);
    if (is_mlp) WAIT_VM(5); else WAIT_VM(1);
    BAR();
    STAMP(5);
    {
      v4u oa[2]; v2u wf[2][2];
#pragma unroll
      for (int i = 0; i < 2; ++i) {
        oa[i] = lds128(L, L_O + (n & 1) * 128 + i * 64 + q * 16);
#pragma unroll
        for (int t = 0; t < 2; ++t) {
          const int row = 32 * i + 8 * q + 4 * t + (n >> 2);
          wf[i][t] = tr_read(L, L_WO + row * 256 + (((2 * wave + ((n >> 1) & 1)) ^ swz(row)) << 4) + (n & 1) * 8);
        }
      }
      f32x4 y = {0.f, 0.f, 0.f, 0.f};
#pragma unroll
      for (int i = 0; i < 2; ++i) y = mfma32(oa[i], v4u{wf[i][0].x, wf[i][0].y, wf[i][1].x, wf[i][1].y}, y);
      const int c = cb * 128 + 16 * wave + n;
      const int pidx = (g * 2 + hp) * 4 + dq;
      const int off = WS_PC + ((c >> 3) * 64 + pidx) * 128 + (c & 7) * 16;
      put(rs, q == 0 ? off : OOB, fu(y[0]), fu(y[1]), tag);
    }
    STAMP(6);
    if (is_mlp) {
      WAIT_VM(2);
      BAR();
      v2u wf[4];
#pragma unroll
      for (int t = 0; t < 4; ++t) {
        const int row = 4 * q + (n >> 2), ct = 4 * wave + t;
        wf[t] = tr_read(L, L_W2 + row * 1024 + (((2 * ct + ((n >> 1) & 1)) ^ ((row & 7) << 1)) << 4) + (n & 1) * 8);
      }
      BAR();
      const v2u ga = *(LAS v2u*)(L + L_G + (n & 1) * 32 + q * 8);
      f32x4 z[4];
#pragma unroll
      for (int t = 0; t < 4; ++t) z[t] = mfma16(ga, wf[t], f32x4{0.f, 0.f, 0.f, 0.f});
      const float z0 = q == 0 ? z[0][0] : q == 1 ? z[1][0] : q == 2 ? z[2][0] : z[3][0];
      const float z1 = q == 0 ? z[0][1] : q == 1 ? z[1][1] : q == 2 ? z[2][1] : z[3][1];
      const int c = 64 * wave + lane;
      put(rs, WS_PD + ((c >> 3) * NM + mi) * 128 + (c & 7) * 16, fu(z0), fu(z1), tag);
      STAMP(7);
    }
  } else {
    v4u xr0 = ldg(p.x, lane * 16), xr1 = ldg(p.x, 1024 + lane * 16);
    v4u er = ldg(p.ws, 0);
#pragma unroll
    for (int i = 0; i < 16; ++i) dma16(p.wq, (32 * i + (lane >> 1)) * 8192 + (g * 512 + 16 * j + 8 * (lane & 1)) * 2, lbase + L_WQ + i * 1024);
    asm volatile("s_waitcnt vmcnt(16)" : "+v"(xr0), "+v"(xr1), "+v"(er)::"memory");
    STAMP(1);
    const unsigned tag = __builtin_amdgcn_readfirstlane(er.x) + 1u;
    if (lane == 0) *(LAS unsigned*)(L + L_TAG) = tag;
    {
      float v[2][8];
#pragma unroll
      for (int i = 0; i < 4; ++i) { v[0][2 * i] = bflo(xr0[i]); v[0][2 * i + 1] = bfhi(xr0[i]); v[1][2 * i] = bflo(xr1[i]); v[1][2 * i + 1] = bfhi(xr1[i]); }
      ln2(v);
#pragma unroll
      for (int bb = 0; bb < 2; ++bb)
        *(LAS v4u*)(L + L_XN + bb * 1024 + lane * 16) = v4u{pk2(v[bb][0], v[bb][1]), pk2(v[bb][2], v[bb][3]), pk2(v[bb][4], v[bb][5]), pk2(v[bb][6], v[bb][7])};
    }
    WAIT_VM(0);
    STAMP(2);
    {
      const f32x4 qv = gemv16(L, L_WQ, lane);
      if (q < 2) *(LAS u16*)(L + L_T + q * 32 + n * 2) = (u16)pk2(q == 0 ? qv[0] : qv[1], 0.f);
      const v2u pr = *(LAS v2u*)(L + L_T + (lane & 7) * 8);
      put(rs, lane < 8 ? WS_QX + (g * 32 + j) * 128 + lane * 16 : OOB, pr.x, pr.y, tag);
    }
    STAMP(3);
    float xres0 = 0.f, xres1 = 0.f, bias = 0.f;
    if (is_red) { const int c = ri * 8 + (lane & 7); xres0 = bf2f(p.x[c]); xres1 = bf2f(p.x[DM + c]); bias = bf2f(p.b2[c]); }
    if (is_mlp) bias = bf2f(p.b1[mi * 16 + n]);
    {
      int off[2]; unsigned a[2], c2[2];
#pragma unroll
      for (int i = 0; i < 2; ++i) off[i] = WS_QX + (g * 32 + 16 * i + (lane >> 2)) * 128 + b * 64 + (lane & 3) * 16;
      gather<2>(rs, off, tag, a, c2);
#pragma unroll
      for (int i = 0; i < 2; ++i) *(LAS v2u*)(L + L_Q + i * 512 + lane * 8) = v2u{a[i], c2[i]};
    }
    STAMP(4);
    BAR();
    BAR();
    BAR();
    {
      const int h = lane & 3;
      const f32x4 m0 = *(LAS f32x4*)(L + L_MAX + h * 32), m1 = *(LAS f32x4*)(L + L_MAX + h * 32 + 16);
      const f32x4 s0 = *(LAS f32x4*)(L + L_SUM + h * 32), s1 = *(LAS f32x4*)(L + L_SUM + h * 32 + 16);
      const float M = fmaxf(fmaxf(fmaxf(m0[0], m0[1]), fmaxf(m0[2], m0[3])), fmaxf(fmaxf(m1[0], m1[1]), fmaxf(m1[2], m1[3])));
      const float S = ((s0[0] + s0[1]) + (s0[2] + s0[3])) + ((s1[0] + s1[1]) + (s1[2] + s1[3]));
      put(rs, lane < 4 ? WS_ML + ((g * 2 + b) * 16 + s) * 128 + lane * 16 : OOB, fu(M), fu(S), tag);
    }
    STAMP(5);
    {
      const int bb = lane >> 5, dd = lane & 31;
      int off[18]; unsigned a[18], c2[18];
#pragma unroll
      for (int i = 0; i < 16; ++i) off[i] = WS_PB + ((((g * 2 + hp) * 2 + bb) * 16 + i) * 128 + 32 * dq + dd) * 16;
      off[16] = WS_ML + ((g * 2 + bb) * 16 + n) * 128 + (2 * hp) * 16;
      off[17] = off[16] + 16;
      gather<18>(rs, off, tag, a, c2);
      STAMP(6);
      float al[2];
#pragma unroll
      for (int hh = 0; hh < 2; ++hh) {
        const float m = uf(a[16 + hh]), l = uf(c2[16 + hh]);
        const float M = row_max(m);
        const float w = __builtin_amdgcn_exp2f((m - M) * 1.4426950408889634f);
        const float Ls = row_sum(w * l);
        al[hh] = w / Ls;
      }
      float o0 = 0.f, o1 = 0.f;
#define ACC(i) o0 = fmaf(dpp<0x150 + i>(al[0]), uf(a[i]), o0); o1 = fmaf(dpp<0x150 + i>(al[1]), uf(c2[i]), o1);
      ACC(0) ACC(1) ACC(2) ACC(3) ACC(4) ACC(5) ACC(6) ACC(7) ACC(8) ACC(9) ACC(10) ACC(11) ACC(12) ACC(13) ACC(14) ACC(15)
#undef ACC
      const unsigned pk = pk2(o0, o1);
      *(LAS u16*)(L + L_O + bb * 128 + dd * 2) = (u16)pk;
      *(LAS u16*)(L + L_O + bb * 128 + 64 + dd * 2) = (u16)(pk >> 16);
    }
    BAR();
    STAMP(7);
    if (is_red) {
      float h0, h1;
      {
        int off[8]; unsigned a[8], c2[8];
#pragma unroll
        for (int i = 0; i < 8; ++i) off[i] = WS_PC + ri * 8192 + i * 1024 + lane * 16;
        gather<8>(rs, off, tag, a, c2);
        STAMP(8);
        float y0 = 0.f, y1 = 0.f;
#pragma unroll
        for (int i = 0; i < 8; ++i) { y0 += uf(a[i]); y1 += uf(c2[i]); }
        y0 += dpp<0x128>(y0); y1 += dpp<0x128>(y1);
        y0 = xrow_sum(y0); y1 = xrow_sum(y1);
        h0 = rbf(xres0 + rbf(y0)); h1 = rbf(xres1 + rbf(y1));
        const unsigned pk = pk2(h0, h1);
        const unsigned pk4 = dppu<0x124>(pk);
        put(rs, lane < 4 ? WS_HX + ri * 128 + lane * 16 : OOB, pk, pk4, tag);
      }
      STAMP(9);
      {
        int off[14]; unsigned a[14], c2[14];
#pragma unroll
        for (int i = 0; i < 14; ++i) off[i] = WS_PD + ri * (NM * 128) + i * 1024 + lane * 16;
        gather<14>(rs, off, tag, a, c2);
        STAMP(10);
        float y0 = 0.f, y1 = 0.f;
#pragma unroll
        for (int i = 0; i < 14; ++i) { y0 += uf(a[i]); y1 += uf(c2[i]); }
        y0 += dpp<0x128>(y0); y1 += dpp<0x128>(y1);
        y0 = xrow_sum(y0); y1 = xrow_sum(y1);
        const float r0 = h0 + rbf(rbf(y0) + bias), r1 = h1 + rbf(rbf(y1) + bias);
        const unsigned pk = pk2(r0, r1);
        if (lane < 16) p.out[(lane >> 3) * DM + ri * 8 + (lane & 7)] = (u16)(lane < 8 ? pk : pk >> 16);
        if (ri == 0 && lane == 0) __builtin_amdgcn_raw_buffer_store_b32(tag, rs, WS_E, 0, 16);
      }
      STAMP(11);
    }
    if (is_mlp) {
      {
        int off[4]; unsigned a[4], c2[4];
#pragma unroll
        for (int i = 0; i < 4; ++i) off[i] = WS_HX + (16 * i + (lane >> 2)) * 128 + (lane & 3) * 16;
        gather<4>(rs, off, tag, a, c2);
        STAMP(8);
        float v[2][8];
#pragma unroll
        for (int i = 0; i < 4; ++i) { v[0][2 * i] = bflo(a[i]); v[1][2 * i] = bfhi(a[i]); v[0][2 * i + 1] = bflo(c2[i]); v[1][2 * i + 1] = bfhi(c2[i]); }
        ln2(v);
#pragma unroll
        for (int i = 0; i < 4; ++i) {
          const int c = (16 * i + (lane >> 2)) * 8 + (lane & 3);
          const unsigned p0 = pk2(v[0][2 * i], v[0][2 * i + 1]), p1 = pk2(v[1][2 * i], v[1][2 * i + 1]);
          *(LAS u16*)(L + L_XN + c * 2) = (u16)p0;
          *(LAS u16*)(L + L_XN + c * 2 + 8) = (u16)(p0 >> 16);
          *(LAS u16*)(L + L_XN + 1024 + c * 2) = (u16)p1;
          *(LAS u16*)(L + L_XN + 1024 + c * 2 + 8) = (u16)(p1 >> 16);
        }
      }
      BAR();
      STAMP(9);
      {
        const f32x4 u4 = gemv16(L, L_W1, lane);
        const float u = rbf(rbf(q == 0 ? u4[0] : u4[1]) + bias);
        const float inner = 0.7978845608028654f * (u + 0.044715f * u * u * u);
        const float e = __builtin_amdgcn_exp2f(inner * 2.8853900817779268f);
        const float th = 1.f - 2.f / (e + 1.f);
        const float gv = 0.5f * u * (1.f + th);
        if (q < 2) *(LAS u16*)(L + L_G + q * 32 + n * 2) = (u16)pk2(gv, gv);
      }
      BAR();
      STAMP(10);
    }
  }
}

extern "C" void kernel_launch(void* const* d_in, const int* in_sizes, int n_in, void* d_out, int out_size, void* d_ws, size_t ws_size,
                              hipStream_t stream) {
  Params p;
  p.x = (const u16*)d_in[0]; p.kc = (const u16*)d_in[1]; p.vc = (const u16*)d_in[2]; p.wq = (const u16*)d_in[3]; p.wo = (const u16*)d_in[4];
  p.w1 = (const u16*)d_in[6]; p.b1 = (const u16*)d_in[7]; p.w2 = (const u16*)d_in[8]; p.b2 = (const u16*)d_in[9];
  p.out = (u16*)d_out; p.ws = (unsigned char*)d_ws;
  (void)hipFuncSetAttribute((const void*)fused_block, hipFuncAttributeMaxDynamicSharedMemorySize, L_END);
  hipLaunchKernelGGL(fused_block, dim3(NWG), dim3(NT), L_END, stream, p);
}
```

```cpp
#include <hip/hip_runtime.h>
#include <hip/hip_bf16.h>
#include <stdint.h>

#define TUNE 0
#define STAMPS 0
#define DLY_A 12
#define DLY_B 6
#define DLY_C1 2
#define DLY_C2 0
#define DLY_D 28
#define DLY_K 10
#define WATCH 1
#define DLY_W 2
#define DLY_M 30
#define PREW 1
#define CODE_BYTES 7680
#define CODEPF 0

typedef unsigned short u16;
typedef unsigned v4u __attribute__((ext_vector_type(4)));
typedef unsigned v2u __attribute__((ext_vector_type(2)));
typedef float f32x4 __attribute__((ext_vector_type(4)));
typedef float f32x2 __attribute__((ext_vector_type(2)));
typedef short s16x4 __attribute__((ext_vector_type(4)));
typedef __bf16 b16x8 __attribute__((ext_vector_type(8)));
typedef __bf16 b16x2 __attribute__((ext_vector_type(2)));
#define LAS __attribute__((address_space(3)))

constexpr int NWG = 256, NT = 512;
constexpr int NM = 112;
constexpr int DM = 512, FF = 1792, SK = 8192, WIN = 2048;
constexpr int PBC = 384;
constexpr int WS_E = 0, WS_HX = 4096, WS_QX = 16384  , WS_PB = WS_QX + 262144;
constexpr int WS_PC = WS_PB + 256 * 8 * PBC, WS_PD = WS_PC + 64 * 64 * 128, WS_END = WS_PD + 64 * NM * 128;
constexpr int WS_DBG = 8 << 20;
constexpr int XS = 1088, QS = 320;
constexpr int L_XN = 0  , L_Q = 3328, L_P = L_Q + 4 * QS, L_MAX = L_P + 4 * QS, L_SUM = L_MAX + 128, L_M = L_SUM + 128  ,
              L_O = L_M + 64, L_G = L_O + 256  , L_RED = L_G + 512  , L_V = 9216, L_K = L_V + 32768, L_WQ = L_K + 32768,
              L_W = L_WQ + 16384  , L_W2 = L_W + 16384, L_END = L_W + 32768;
static_assert(L_RED + 2048 <= L_V && L_RED % 16 == 0 && L_O % 16 == 0, "LDS map");
static_assert(L_END > 81920, "one workgroup per CU");
constexpr int OOB = 0x7ffffff0;
constexpr int SPIN_CAP = 1 << 16;
constexpr float LOG2E = 1.4426950408889634f;

struct Params { const u16* x; unsigned char* ws; const u16 *wq, *kc, *vc, *wo, *w1, *w2, *b1, *b2; u16* out;
};
#define NAP(name, idx) nap<name>()

__device__ __forceinline__ unsigned fu(float f) { return __float_as_uint(f); }
__device__ __forceinline__ float uf(unsigned u) { return __uint_as_float(u); }
__device__ __forceinline__ float bflo(unsigned w) { return uf(w << 16); }
__device__ __forceinline__ float bfhi(unsigned w) { return uf(w & 0xffff0000u); }
__device__ __forceinline__ unsigned pk2(float lo, float hi) {
  f32x2 v = {lo, hi};
  return __builtin_bit_cast(unsigned, __builtin_convertvector(v, b16x2));
}
__device__ __forceinline__ float rbf(float f) { return bflo(pk2(f, f)); }
__device__ __forceinline__ float dot2(unsigned a, unsigned b, float c) {
  return __builtin_amdgcn_fdot2_f32_bf16(__builtin_bit_cast(b16x2, a), __builtin_bit_cast(b16x2, b), c, false);
}
template <int CTRL> __device__ __forceinline__ float dpp(float x) { return uf((unsigned)__builtin_amdgcn_update_dpp(0, (int)fu(x), CTRL, 0xf, 0xf, true)); }
template <int CTRL> __device__ __forceinline__ unsigned dppu(unsigned x) { return (unsigned)__builtin_amdgcn_update_dpp(0, (int)x, CTRL, 0xf, 0xf, true); }
constexpr int ROR1 = 0x121, ROR2 = 0x122, ROR4 = 0x124, ROR8 = 0x128, XOR2 = 0x4E;
__device__ __forceinline__ float fmx(float a, float b) { return __builtin_elementwise_maximum(a, b); }
__device__ __forceinline__ float row_max(float v) {
  float r;
  asm volatile("s_nop 1\n\tv_max_f32_dpp %0, %1, %1 row_ror:8 row_mask:0xf bank_mask:0xf\n\ts_nop 1\n\tv_max_f32_dpp %0, %0, %0 row_ror:4 row_mask:0xf bank_mask:0xf\n\t"
               "s_nop 1\n\tv_max_f32_dpp %0, %0, %0 row_ror:2 row_mask:0xf bank_mask:0xf\n\ts_nop 1\n\tv_max_f32_dpp %0, %0, %0 row_ror:1 row_mask:0xf bank_mask:0xf"
               : "=&v"(r) : "v"(v));
  return r;
}
__device__ __forceinline__ float row_sum(float v) { v += dpp<ROR8>(v); v += dpp<ROR4>(v); v += dpp<ROR2>(v); v += dpp<ROR1>(v); return v; }
__device__ __forceinline__ float tsum32(float a, float b) { auto t = __builtin_amdgcn_permlane32_swap(fu(a), fu(b), false, false); return uf(t[0]) + uf(t[1]); }
__device__ __forceinline__ float tsum16(float a, float b) { auto t = __builtin_amdgcn_permlane16_swap(fu(a), fu(b), false, false); return uf(t[0]) + uf(t[1]); }
__device__ __forceinline__ float xrow_sum(float x) { x = tsum16(x, x); return tsum32(x, x); }
__device__ __forceinline__ float xrow_max(float x) {
  auto s = __builtin_amdgcn_permlane16_swap(fu(x), fu(x), false, false); x = fmx(uf(s[0]), uf(s[1]));
  auto t = __builtin_amdgcn_permlane32_swap(fu(x), fu(x), false, false); return fmx(uf(t[0]), uf(t[1]));
}
__device__ __forceinline__ f32x4 mfma32(v4u a, v4u b, f32x4 c) {
  return __builtin_amdgcn_mfma_f32_16x16x32_bf16(__builtin_bit_cast(b16x8, a), __builtin_bit_cast(b16x8, b), c, 0, 0, 0);
}
__device__ __forceinline__ f32x4 mfma16(v2u a, v2u b, f32x4 c) {
  return __builtin_amdgcn_mfma_f32_16x16x16bf16_1k(__builtin_bit_cast(s16x4, a), __builtin_bit_cast(s16x4, b), c, 0, 0, 0);
}
__device__ __forceinline__ v2u tr_read(LAS unsigned char* L, int off) {
  return __builtin_bit_cast(v2u, __builtin_amdgcn_ds_read_tr16_b64_v4i16((LAS s16x4*)(L + off)));
}
__device__ __forceinline__ v4u cat(v2u a, v2u b) { return v4u{a.x, a.y, b.x, b.y}; }
__device__ __forceinline__ v4u lds128(LAS unsigned char* L, int off) { return *(LAS v4u*)(L + off); }
__device__ __forceinline__ f32x4 ldsf4(LAS unsigned char* L, int off) { return *(LAS f32x4*)(L + off); }
__device__ __forceinline__ v4u ldg128(const void* base, int voff) {
  v4u r; asm volatile("global_load_dwordx4 %0, %1, %2" : "=v"(r) : "v"(voff), "s"(base) : "memory"); return r;
}
template <int OFS> __device__ __forceinline__ v4u ldg128nt(const void* base, int voff) {
  v4u r; asm volatile("global_load_dwordx4 %0, %1, %2 offset:%3 nt" : "=v"(r) : "v"(voff), "s"(base), "i"(OFS) : "memory"); return r;
}
__device__ __forceinline__ unsigned ldg32(const void* base, int voff) {
  unsigned r; asm volatile("global_load_dword %0, %1, %2" : "=v"(r) : "v"(voff), "s"(base) : "memory"); return r;
}
__device__ __forceinline__ unsigned ldg16(const void* base, int voff) {
  unsigned r; asm volatile("global_load_ushort %0, %1, %2" : "=v"(r) : "v"(voff), "s"(base) : "memory"); return r;
}
__device__ __forceinline__ void dma16(const void* base, int voff, unsigned lds_dst) {
  asm volatile("s_mov_b32 m0, %2\n\ts_nop 0\n\tglobal_load_lds_dwordx4 %0, %1 nt" ::"v"(voff), "s"(base), "s"(lds_dst) : "memory");
}
#define WAIT_VM(n) asm volatile("s_waitcnt vmcnt(" #n ")" ::: "memory")
#define BAR() asm volatile("s_waitcnt lgkmcnt(0)\n\ts_barrier" ::: "memory")
#define SCHED() __builtin_amdgcn_sched_barrier(0)
#define PIN(x) asm volatile("" : "+v"(x))
template <int N> __device__ __forceinline__ void nap() {
  if (N > 0) __builtin_amdgcn_s_sleep(N > 127 ? 127 : N);
}
__device__ __forceinline__ int swz(int row) { return ((row & 3) << 2) | ((row >> 2) & 3); }

__device__ __forceinline__ void put(__amdgpu_buffer_rsrc_t rs, int off, unsigned a, unsigned b, unsigned tag) {
  v4u v = {a, tag, b, tag};
  __builtin_amdgcn_raw_buffer_store_b128(v, rs, off, 0, 16);
}

template <int N, int STRIDE> __device__ __forceinline__ int gather(__amdgpu_buffer_rsrc_t rs, int off, unsigned tag, v4u (&v)[N]) {
  int spin = 0;
#pragma nounroll
  for (; spin < SPIN_CAP; ++spin) {
#pragma unroll
    for (int k = 0; k < N; ++k) v[k] = __builtin_amdgcn_raw_buffer_load_b128(rs, off, k * STRIDE, 16);
    SCHED();
    bool bad = false;
#pragma unroll
    for (int k = 0; k < N; ++k) bad = bad || (v[k].y != tag) || (v[k].w != tag);
    if (__builtin_amdgcn_ballot_w64(bad) == 0ull) break;
    asm volatile("" ::: "memory");
  }
  return spin + 1;
}

#define STAMP(i) do { } while (0)
#define CNT(i, x) do { (void)(x); } while (0)

__global__ void __launch_bounds__(NT) fused_block(Params p) {
  extern __shared__ __attribute__((aligned(16))) unsigned char smem[];
  LAS unsigned char* L = (LAS unsigned char*)smem;
  const unsigned lbase = (unsigned)(size_t)L;
  const int tid = threadIdx.x, lane = tid & 63, wave = __builtin_amdgcn_readfirstlane(tid >> 6);
  const int bid = blockIdx.x, g = bid & 7, j = bid >> 3, b = j >> 4, s = j & 15;
  const int q = lane >> 4, n = lane & 15, par = lane & 1;
  STAMP(0);
  unsigned cpf = 0;
  asm volatile("" ::"s"(p.b1), "s"(p.b2), "s"(p.out), "s"(p.x));
  const int kq = j >> 3, c64 = j & 7;
  v4u xs0 = {0u, 0u, 0u, 0u}, xs1 = xs0; v2u xp = {0u, 0u}; unsigned er = 0;
  if (wave == 0) {
    xs0 = ldg128(p.x, par * 1024 + (lane >> 1) * 32); xs1 = ldg128(p.x, par * 1024 + (lane >> 1) * 32 + 16);
    asm volatile("global_load_dwordx2 %0, %1, %2" : "=v"(xp) : "v"(par * 1024 + kq * 256 + (lane >> 1) * 8), "s"(p.x) : "memory");
    er = ldg32(p.ws, 0);
  }
#pragma unroll
  for (int i = 0; i < 2; ++i) {
    const int r = 16 * wave + 8 * i + (lane >> 3);
    dma16(p.wq, (128 * kq + r) * 8192 + g * 1024 + c64 * 128 + (((lane & 7) ^ ((((r >> 1) & 1) | ((r >> 2) & 2)) << 1)) << 4), lbase + L_WQ + wave * 2048 + i * 1024);
  }
  const size_t kvoff = ((size_t)(b * 8 + g) * SK + (SK - WIN)) * 256 + s * 1024;
  const unsigned char* kb = (const unsigned char*)p.kc + kvoff;
  const unsigned char* vb = (const unsigned char*)p.vc + kvoff;
#define KLD(i) dma16(kb, ((i) * 8 + wave) * 16384 + q * 256 + ((n ^ (4 * (i) + q)) << 4), lbase + L_K + wave * 4096 + (i) * 1024)
  if (wave >= 4) { NAP(DLY_K, 0); KLD(0); KLD(1); KLD(2); KLD(3); }
  const bool is_wo = j < 16, is_mlp = (j >= 16) && (j < 30), is_red = j < 8;
  const int d16 = j >> 1, ch = j & 1;
  const int mi = g * 14 + (j - 16), ri = g * 8 + j;
  const __amdgpu_buffer_rsrc_t rs = __builtin_amdgcn_make_buffer_rsrc((void*)p.ws, 0, WS_END, 0x00020000);
  int off_pb = (n < 4 || lane < 8) ? WS_PB + (((g * 2 + b) * 16 + s) * 8 + wave) * PBC + (n < 4 ? q * 64 + n * 16 : 256 + (n - 4) * 16) : OOB;
  const int col_c = ch * 256 + q * 128 + 16 * wave + n;
  int off_p2 = is_wo ? (q < 2 ? WS_PC + ((col_c >> 3) * 64 + g * 8 + d16) * 128 + (col_c & 7) * 16 : OOB)
                     : (is_mlp ? WS_PD + ((8 * wave + (lane >> 3)) * NM + mi) * 128 + (lane & 7) * 16 : OOB);
  int off_q = (q < 2 && n < 8) ? WS_QX + (((g * 2 + q) * 4 + kq) * 32 + c64 * 4 + wave) * 128 + n * 16 : OOB;
  int tr0 = (8 * q + (n >> 2)) * 256 + (n & 1) * 8 + (((2 * wave + ((n >> 1) & 1)) ^ swz(8 * q + (n >> 2))) << 4);
  int tr1 = (8 * q + (n >> 2)) * 256 + (n & 1) * 8 + (((2 * wave + ((n >> 1) & 1)) ^ swz(8 * q + 4 + (n >> 2))) << 4) + 1024;
  int wo0 = ((g * 4 + (wave & 3)) * 128 + 16 * d16 + q) * 1024 + ch * 512 + (wave >> 2) * 256;
  int wo1 = wo0 + 4096 + ((n ^ swz(4 + q)) << 4), wo2 = wo0 + 8192 + ((n ^ swz(8 + q)) << 4), wo3 = wo0 + 12288 + ((n ^ swz(12 + q)) << 4);
  wo0 += (n ^ swz(q)) << 4;
  int a_q = L_Q + (n >> 2) * QS + q * 16, a_p = L_P + (n & 3) * QS + q * 16, a_pw = L_P + q * QS + (16 * wave + n) * 2;
  int a_mx = L_MAX + q * 32, a_mw = a_mx + wave * 4, a_s = L_SUM + (n & 3) * 32, a_m = L_M + (n & 3) * 4;
  PIN(off_pb); PIN(off_p2); PIN(off_q); PIN(tr0); PIN(tr1); PIN(wo0); PIN(wo1); PIN(wo2); PIN(wo3);
  PIN(a_q); PIN(a_p); PIN(a_pw); PIN(a_mx); PIN(a_mw); PIN(a_s); PIN(a_m);
  *(LAS unsigned*)(L + L_XN + 2 * XS + (tid & 255) * 4) = 0x3F803F80u;
  *(LAS unsigned*)(L + L_M + 24 + par * 4) = 0u;
  if (wave == 0) {
    asm volatile("s_waitcnt vmcnt(2)" : "+v"(xs0), "+v"(xs1), "+v"(xp), "+v"(er)::"memory");
    STAMP(1);
    *(LAS unsigned*)(L + L_M + 16) = er + 1u;
    float s1 = 0.f, s2 = 0.f;
#pragma unroll
    for (int i = 0; i < 4; ++i) {
      s1 = dot2(xs0[i], 0x3F803F80u, s1); s2 = dot2(xs0[i], xs0[i], s2);
      s1 = dot2(xs1[i], 0x3F803F80u, s1); s2 = dot2(xs1[i], xs1[i], s2);
    }
    s1 += dpp<XOR2>(s1); s2 += dpp<XOR2>(s2);
    s1 += dpp<ROR4>(s1); s2 += dpp<ROR4>(s2);
    s1 += dpp<ROR8>(s1); s2 += dpp<ROR8>(s2);
    s1 = xrow_sum(s1); s2 = xrow_sum(s2);
    const float mean = s1 * (1.f / DM);
    const float var = fmaxf(fmaf(-mean, mean, s2 * (1.f / DM)), 0.f);
    const float mb = rbf(mean);
    const float rden = __builtin_amdgcn_rcpf(rbf(__builtin_amdgcn_sqrtf(rbf(rbf(var) + 1e-5f))));
    const unsigned d0 = pk2(bflo(xp.x) - mb, bfhi(xp.x) - mb), d1 = pk2(bflo(xp.y) - mb, bfhi(xp.y) - mb);
    *(LAS v2u*)(L + L_XN + par * XS + (lane >> 1) * 8) = v2u{pk2(bflo(d0) * rden, bfhi(d0) * rden), pk2(bflo(d1) * rden, bfhi(d1) * rden)};
  }
  if (wave >= 4) WAIT_VM(4); else WAIT_VM(0);
  PIN(cpf);
  STAMP(2);
  BAR();
  const unsigned tag = __builtin_amdgcn_readfirstlane(*(LAS unsigned*)(L + L_M + 16));
  if (wave < 4) {
    {
      const int aoff = L_XN + (n & 1) * XS + q * 16;
      const int r0 = 8 * q + (n >> 2);
      const int sw = (((r0 >> 1) & 1) | ((r0 >> 2) & 2)) << 1;
      const int boff = L_WQ + r0 * 128 + (((2 * wave + ((n >> 1) & 1)) ^ sw) << 4) + (n & 1) * 8;
      v4u a[4]; v2u bw[4][2];
#pragma unroll
      for (int i = 0; i < 4; ++i) { a[i] = lds128(L, aoff + i * 64); bw[i][0] = tr_read(L, boff + i * 4096); bw[i][1] = tr_read(L, boff + i * 4096 + 512); }
      SCHED();
      f32x4 acc = {0.f, 0.f, 0.f, 0.f};
#pragma unroll
      for (int i = 0; i < 4; ++i) acc = mfma32(a[i], cat(bw[i][0], bw[i][1]), acc);
      const float v = q ? acc[1] : acc[0];
      put(rs, off_q, fu(v), fu(dpp<ROR8>(v)), tag);
    }
    STAMP(3);
    KLD(0); KLD(1); KLD(2); KLD(3);
    put(rs, off_pb, 0u, 0u, tag - 1u); put(rs, off_p2, 0u, 0u, tag - 1u);
    int qw = L_Q + wave * QS + (lane >> 3) * 32 + (lane & 7) * 2;
    PIN(qw);
    NAP(DLY_A, 1);
    {
      v4u v[4];
      CNT(28, (gather<4, 4096>(rs, WS_QX + (g * 2 + b) * 16384 + tid * 16, tag, v)));
      const unsigned pk = pk2((uf(v[0].x) + uf(v[1].x)) + (uf(v[2].x) + uf(v[3].x)), (uf(v[0].z) + uf(v[1].z)) + (uf(v[2].z) + uf(v[3].z)));
      *(LAS u16*)(L + qw) = (u16)pk;
      *(LAS u16*)(L + qw + 16) = (u16)(pk >> 16);
    }
  } else {
    const int voff = (wave - 4) * 32768 + q * 256;
    const unsigned vdst = lbase + L_V + (wave - 4) * 8192;
#define VLD(t) dma16(vb, voff + (((t) >> 1) * 8 + ((t) & 1)) * 16384 + ((n ^ swz(4 * ((t) >> 1) + q)) << 4), vdst + ((t) & 1) * 4096 + ((t) >> 1) * 1024)
    WAIT_VM(3); VLD(0);
    WAIT_VM(3); VLD(1);
    WAIT_VM(3); VLD(2);
    WAIT_VM(3); VLD(3);
    WAIT_VM(3); VLD(4); VLD(5);
    WAIT_VM(4); VLD(6); VLD(7);
#undef VLD
    put(rs, off_pb, 0u, 0u, tag - 1u); put(rs, off_p2, 0u, 0u, tag - 1u);
  }
  v4u kf[4];
#pragma unroll
  for (int i = 0; i < 4; ++i) kf[i] = lds128(L, L_K + wave * 4096 + n * 256 + (((4 * i + q) ^ n) << 4));
  STAMP(4);
  BAR();
  STAMP(5);
  float sc;
  {
    v4u qa[4];
#pragma unroll
    for (int i = 0; i < 4; ++i) qa[i] = lds128(L, a_q + i * 64);
    SCHED();
    f32x4 acc = {0.f, 0.f, 0.f, 0.f};
#pragma unroll
    for (int i = 0; i < 4; ++i) acc = mfma32(qa[i], kf[i], acc);
    SCHED();
    if (is_wo) { dma16(p.wo, wo0, lbase + L_W + wave * 4096); dma16(p.wo, wo1, lbase + L_W + wave * 4096 + 1024); }
    SCHED();
    sc = rbf(rbf(acc[0]) * 0.08838834764831845f);
    *(LAS float*)(L + a_mw) = row_max(sc);
  }
  BAR();
  {
    const f32x4 m0 = ldsf4(L, a_mx), m1 = ldsf4(L, a_mx + 16);
    SCHED();
    if (is_wo) { dma16(p.wo, wo2, lbase + L_W + wave * 4096 + 2048); dma16(p.wo, wo3, lbase + L_W + wave * 4096 + 3072); }
    SCHED();
    const float M = fmx(fmx(fmx(fmx(m0[0], m0[1]), m0[2]), fmx(fmx(m0[3], m1[0]), m1[1])), fmx(m1[2], m1[3]));
    const float pr = __builtin_amdgcn_exp2f((sc - M) * LOG2E);
    *(LAS u16*)(L + a_pw) = (u16)pk2(pr, pr);
    *(LAS float*)(L + L_M + q * 4) = M;
    *(LAS float*)(L + a_mw + 128) = row_sum(pr);
  }
  if (is_wo) WAIT_VM(4); else WAIT_VM(0);
  STAMP(6);
  BAR();
  {
    v4u pa[4]; v2u vf[4][2];
#pragma unroll
    for (int i = 0; i < 4; ++i) {
      pa[i] = lds128(L, a_p + i * 64);
      vf[i][0] = tr_read(L, L_V + i * 8192 + tr0);
      vf[i][1] = tr_read(L, L_V + i * 8192 + tr1);
    }
    const f32x4 s0 = ldsf4(L, a_s), s1 = ldsf4(L, a_s + 16);
    float Mh = *(LAS float*)(L + a_m);
    SCHED();
    f32x4 o = {0.f, 0.f, 0.f, 0.f};
#pragma unroll
    for (int i = 0; i < 4; ++i) o = mfma32(cat(vf[i][0], vf[i][1]), pa[i], o);
    const f32x4 ss = s0 + s1;
    float Ls = (ss[0] + ss[1]) + (ss[2] + ss[3]);
    PIN(Ls); PIN(Mh);
    unsigned o01 = pk2(o[0], o[1]), o23 = pk2(o[2], o[3]);
    PIN(o01); PIN(o23);
    put(rs, off_pb, n < 4 ? o01 : fu(Mh), n < 4 ? o23 : fu(Ls), tag);
  }
  STAMP(7);
  if (is_wo) {
    unsigned sm0 = 0, sm1 = 0;
    if (is_red) { const int c = ri * 8 + (lane & 7); sm0 = ldg16(p.x, ((lane >> 5) * DM + c) * 2); sm1 = ldg16(p.b2, c * 2); }
    const int bb = wave >> 2, dqd = wave & 3;
    int base = WS_PB + (((g * 2 + bb) * 16 + (lane >> 2)) * 8 + d16) * PBC + (lane & 3) * 16;
    int lo_ = L_O + bb * 128 + ((lane & 3) * 16 + dqd * 4 + q) * 2;
    PIN(base); PIN(lo_);
    asm volatile("s_waitcnt vmcnt(0)" : "+v"(sm0), "+v"(sm1)::"memory");
    NAP(DLY_B, 2);
    {
      v4u v[2];
      int spin = 0;
#pragma nounroll
      for (; spin < SPIN_CAP; ++spin) {
        v[0] = __builtin_amdgcn_raw_buffer_load_b128(rs, base, dqd * 64, 16);
        v[1] = __builtin_amdgcn_raw_buffer_load_b128(rs, base, 256, 16);
        SCHED();
        const bool bad = (v[0].y != tag) || (v[0].w != tag) || (v[1].y != tag) || (v[1].w != tag);
        if (__builtin_amdgcn_ballot_w64(bad) == 0ull) break;
        asm volatile("" ::: "memory");
      }
      CNT(29, spin + 1);
      STAMP(8);
      const float m = uf(v[1].x);
      float M = fmx(m, dpp<ROR4>(m)); M = fmx(M, dpp<ROR8>(M)); M = xrow_max(M);
      const float w = __builtin_amdgcn_exp2f((m - M) * LOG2E);
      float Ls = w * uf(v[1].z);
      Ls += dpp<ROR4>(Ls); Ls += dpp<ROR8>(Ls); Ls = xrow_sum(Ls);
      const float e0 = tsum32(w * bflo(v[0].x), w * bflo(v[0].z));
      const float e1 = tsum32(w * bfhi(v[0].x), w * bfhi(v[0].z));
      float e = tsum16(e0, e1);
      e += dpp<ROR4>(e); e += dpp<ROR8>(e);
      const float o = e * __builtin_amdgcn_rcpf(Ls);
      *(LAS u16*)(L + lo_) = (u16)pk2(o, o);
    }
    BAR();
    STAMP(9);
    {
      const v4u oa0 = lds128(L, L_O + (n & 1) * 128 + q * 16), oa1 = lds128(L, L_O + (n & 1) * 128 + 64 + q * 16);
      v2u w[2][2][2];
#pragma unroll
      for (int hh = 0; hh < 2; ++hh)
#pragma unroll
        for (int i = 0; i < 2; ++i) { w[hh][i][0] = tr_read(L, L_W + hh * 16384 + i * 8192 + tr0); w[hh][i][1] = tr_read(L, L_W + hh * 16384 + i * 8192 + tr1); }
      SCHED();
      f32x4 y0 = mfma32(oa0, cat(w[0][0][0], w[0][0][1]), f32x4{0.f, 0.f, 0.f, 0.f});
      f32x4 y1 = mfma32(oa0, cat(w[1][0][0], w[1][0][1]), f32x4{0.f, 0.f, 0.f, 0.f});
      y0 = mfma32(oa1, cat(w[0][1][0], w[0][1][1]), y0);
      y1 = mfma32(oa1, cat(w[1][1][0], w[1][1][1]), y1);
      put(rs, off_p2, fu(q ? y1[0] : y0[0]), fu(q ? y1[1] : y0[1]), tag);
    }
    STAMP(10);
    if (is_red && wave == 0) {
      WAIT_VM(0);
      NAP(DLY_C1, 3);
      float h;
      {
        v4u v[8];
        CNT(30, (gather<8, 1024>(rs, WS_PC + ri * 8192 + lane * 16, tag, v)));
        STAMP(11);
        float y0 = 0.f, y1 = 0.f;
#pragma unroll
        for (int i = 0; i < 8; ++i) { y0 += uf(v[i].x); y1 += uf(v[i].z); }
        float y = tsum32(y0, y1);
        y = tsum16(y, y);
        y += dpp<ROR8>(y);
        h = rbf(bflo(sm0) + rbf(y));
        auto t = __builtin_amdgcn_permlane32_swap(fu(h), fu(h), false, false);
        const unsigned pk = (t[0] >> 16) | (t[1] & 0xffff0000u);
        put(rs, lane < 4 ? WS_HX + ri * 128 + lane * 16 : OOB, pk, dppu<ROR4>(pk), tag);
      }
      STAMP(12);
      {
        WAIT_VM(0);
        NAP(DLY_W, 5);
        v4u v[1];
        gather<1, 0>(rs, WS_HX + lane * 128, tag, v);
      }
      NAP(DLY_D, 6);
      {
        v4u v[14];
        CNT(31, (gather<14, 1024>(rs, WS_PD + ri * (NM * 128) + lane * 16, tag, v)));
        STAMP(13);
        float y0 = 0.f, y1 = 0.f;
#pragma unroll
        for (int i = 0; i < 14; ++i) { y0 += uf(v[i].x); y1 += uf(v[i].z); }
        float y = tsum32(y0, y1);
        y = tsum16(y, y);
        y += dpp<ROR8>(y);
        const float r = h + rbf(rbf(y) + bflo(sm1));
        if ((lane & 31) < 8) p.out[(lane >> 5) * DM + ri * 8 + (lane & 7)] = (u16)pk2(r, r);
        if (ri == 0 && lane == 0) __builtin_amdgcn_raw_buffer_store_b32(tag, rs, WS_E, 0, 16);
      }
      STAMP(14);
    }
  } else if (is_mlp) {
    NAP(DLY_M, 7);
#pragma unroll
    for (int i = 0; i < 2; ++i) dma16(p.w1, (64 * wave + 32 * i + (lane >> 1)) * (FF * 2) + mi * 32 + par * 16, lbase + L_W + wave * 2048 + i * 1024);
#pragma unroll
    for (int i = 0; i < 2; ++i) { const int row = 2 * wave + i; dma16(p.w2, (mi * 16 + row) * 1024 + ((lane ^ ((row & 7) << 1)) << 4), lbase + L_W2 + row * 1024); }
    unsigned sm0 = ldg16(p.b1, (mi * 16 + n) * 2);
    asm volatile("s_waitcnt vmcnt(0)" : "+v"(sm0)::"memory");
    BAR();
    STAMP(8);
    const int e_ = n >> 2;
    const int boff = L_W + wave * 2048 + (8 * q + (e_ >> 1) + 4 * (e_ & 1)) * 32 + (n & 3) * 8;
    v2u b00 = tr_read(L, boff), b01 = tr_read(L, boff + 64), b10 = tr_read(L, boff + 1024), b11 = tr_read(L, boff + 1088);
    v2u wf[4];
    {
      const int row = 4 * q + (n >> 2);
#pragma unroll
      for (int tt = 0; tt < 4; ++tt) wf[tt] = tr_read(L, L_W2 + row * 1024 + (((8 * wave + 2 * tt + ((n >> 1) & 1)) ^ ((row & 7) << 1)) << 4) + (n & 1) * 8);
    }
    int hoff = WS_HX + (8 * wave + ((lane >> 2) & 7)) * 128 + (lane & 3) * 16;
    int xw = L_XN + (lane >> 5) * XS + wave * 128 + (lane & 31) * 4;
    int aoff = L_XN + (n < 2 ? n : 2) * XS + wave * 128 + q * 16;
    int rw = L_RED + wave * 256 + n * 16, rr = L_RED + lane * 16;
    int gw = L_G + wave * 64 + (lane & 31) * 2;
    int ga[4];
#pragma unroll
    for (int tt = 0; tt < 4; ++tt) { ga[tt] = (n >> 2) == tt ? L_G + wave * 64 + (n & 1) * 32 + q * 8 : L_M + 24; PIN(ga[tt]); }
    float bias = bflo(sm0);
    PIN(rr); PIN(gw); PIN(bias);
    unsigned psel = lane < 32 ? 0x05040100u : 0x07060302u;
    PIN(b00); PIN(b01); PIN(b10); PIN(b11); PIN(wf[0]); PIN(wf[1]); PIN(wf[2]); PIN(wf[3]); PIN(hoff); PIN(xw); PIN(aoff); PIN(rw); PIN(psel);
    NAP(DLY_C2, 4);
    {
      v4u v[1];
      CNT(30, (gather<1, 0>(rs, hoff, tag, v)));
      STAMP(11);
      *(LAS unsigned*)(L + xw) = __builtin_amdgcn_perm(v[0].z, v[0].x, psel);
    }
    {
      const v4u a0 = lds128(L, aoff), a1 = lds128(L, aoff + 64);
      SCHED();
      f32x4 u = mfma32(a0, cat(b00, b01), f32x4{0.f, 0.f, 0.f, 0.f});
      f32x4 G = mfma32(a0, a0, f32x4{0.f, 0.f, 0.f, 0.f});
      u = mfma32(a1, cat(b10, b11), u);
      G = mfma32(a1, a1, G);
      u[3] = (n & 1) ? G[1] : G[0];
      if (q == 0) *(LAS f32x4*)(L + rw) = u;
    }
    BAR();
    STAMP(12);
    {
      const f32x4 r0 = ldsf4(L, rr), r1 = ldsf4(L, rr + 1024);
      SCHED();
      const f32x4 t = r0 + r1;
      float ub = tsum16(t[0], t[1]); ub = tsum32(ub, ub);
      float cs = tsum32(t[2], t[3]); cs = tsum16(cs, cs);
      const float q0 = uf(__builtin_amdgcn_readlane(fu(cs), 32)), q1 = uf(__builtin_amdgcn_readlane(fu(cs), 33));
      const float m0 = uf(__builtin_amdgcn_readlane(fu(cs), 34)), m1 = uf(__builtin_amdgcn_readlane(fu(cs), 35));
      const bool b1_ = q & 1;
      const float mean = (b1_ ? m1 : m0) * (1.f / DM);
      const float var = fmaxf(fmaf(-mean, mean, (b1_ ? q1 : q0) * (1.f / DM)), 0.f);
      const float mb = rbf(mean);
      const float rden = __builtin_amdgcn_rcpf(rbf(__builtin_amdgcn_sqrtf(rbf(rbf(var) + 1e-5f))));
      const float u = rbf(rbf(fmaf(-mb, cs, ub) * rden) + bias);
      const float inner = u * fmaf(u * u, 0.044715f * 0.7978845608028654f * 2.f * LOG2E, 0.7978845608028654f * 2.f * LOG2E);
      const float gv = u - u * __builtin_amdgcn_rcpf(__builtin_amdgcn_exp2f(inner) + 1.f);
      if (lane < 32) *(LAS u16*)(L + gw) = (u16)pk2(gv, gv);
    }
    {
      v2u gf[4];
#pragma unroll
      for (int tt = 0; tt < 4; ++tt) gf[tt] = *(LAS v2u*)(L + ga[tt]);
      SCHED();
      f32x4 z0 = mfma16(gf[0], wf[0], f32x4{0.f, 0.f, 0.f, 0.f}), z1 = mfma16(gf[1], wf[1], f32x4{0.f, 0.f, 0.f, 0.f});
      z0 = mfma16(gf[2], wf[2], z0); z1 = mfma16(gf[3], wf[3], z1);
      put(rs, off_p2, fu(z0[0] + z1[0]), fu(z0[1] + z1[1]), tag);
    }
    STAMP(13);
  }
}

extern "C" void kernel_launch(void* const* d_in, const int* in_sizes, int n_in, void* d_out, int out_size, void* d_ws, size_t ws_size,
                              hipStream_t stream) {
  Params p;
  p.x = (const u16*)d_in[0]; p.kc = (const u16*)d_in[1]; p.vc = (const u16*)d_in[2]; p.wq = (const u16*)d_in[3]; p.wo = (const u16*)d_in[4];
  p.w1 = (const u16*)d_in[6]; p.b1 = (const u16*)d_in[7]; p.w2 = (const u16*)d_in[8]; p.b2 = (const u16*)d_in[9];
  p.out = (u16*)d_out; p.ws = (unsigned char*)d_ws;
  (void)hipFuncSetAttribute((const void*)fused_block, hipFuncAttributeMaxDynamicSharedMemorySize, L_END);
  hipLaunchKernelGGL(fused_block, dim3(NWG), dim3(NT), L_END, stream, p);
}
```

```cpp
#include <hip/hip_runtime.h>
#include <hip/hip_bf16.h>
#include <stdint.h>

#define DLY_A 9
#define DLY_B 8
#define DLY_C1 5
#define DLY_D 36
#define DLY_K 10
#define DLY_M 6
#define DLY_V 2
#define NVJ 16
#define DLY_H 3

typedef unsigned short u16;
typedef unsigned v4u __attribute__((ext_vector_type(4)));
typedef unsigned v2u __attribute__((ext_vector_type(2)));
typedef float f32x4 __attribute__((ext_vector_type(4)));
typedef float f32x2 __attribute__((ext_vector_type(2)));
typedef short s16x4 __attribute__((ext_vector_type(4)));
typedef __bf16 b16x8 __attribute__((ext_vector_type(8)));
typedef __bf16 b16x2 __attribute__((ext_vector_type(2)));
#define LAS __attribute__((address_space(3)))

constexpr int NWG = 256, NT = 512;
constexpr int NF = 56;
constexpr int DM = 512, FF = 1792, SK = 8192, WIN = 2048;
constexpr int WS_E = 0, WS_V = 4096, WS_QX = 16384  ;
constexpr int WS_PB = 2 << 20  , WS_PC = 4 << 20  ,
              WS_PD = WS_PC + 32 * 16384  , WS_END = WS_PD + 32 * 16384;
constexpr int PD_HX = 7168, PD_PR = 7424;
constexpr int XS = 1088, QS = 320;
constexpr int L_XN = 0  , L_Q = 3328, L_P = L_Q + 4 * QS, L_MAX = L_P + 4 * QS, L_M = L_MAX + 128  , L_Z = L_M + 16  ,
              L_S = L_Z + 16  , L_O = L_Z + 48, L_G = L_O + 256  , L_RED = L_G + 1024  , L_V = 9216, L_K = L_V + 32768, L_WQ = L_K + 32768  ,
              L_W = L_WQ + 16384  , L_END = L_W + 32768;
static_assert(L_RED + 1024 <= L_V && L_RED % 16 == 0 && L_O % 16 == 0 && L_Z % 16 == 0, "LDS map");
static_assert(L_END > 81920, "one workgroup per CU");
constexpr int OOB = 0x7ffffff0;
constexpr int SPIN_CAP = 1 << 20;
constexpr float LOG2E = 1.4426950408889634f;

struct Params { const u16* x; unsigned char* ws; const u16 *wq, *kc, *vc, *wo, *w1, *w2, *b1, *b2; u16* out; };

__device__ __forceinline__ unsigned fu(float f) { return __float_as_uint(f); }
__device__ __forceinline__ float uf(unsigned u) { return __uint_as_float(u); }
__device__ __forceinline__ float bflo(unsigned w) { return uf(w << 16); }
__device__ __forceinline__ float bfhi(unsigned w) { return uf(w & 0xffff0000u); }
__device__ __forceinline__ unsigned pk2(float lo, float hi) {
  f32x2 v = {lo, hi};
  return __builtin_bit_cast(unsigned, __builtin_convertvector(v, b16x2));
}
__device__ __forceinline__ float rbf(float f) { return bflo(pk2(f, f)); }
__device__ __forceinline__ float dot2(unsigned a, unsigned b, float c) {
  return __builtin_amdgcn_fdot2_f32_bf16(__builtin_bit_cast(b16x2, a), __builtin_bit_cast(b16x2, b), c, false);
}
template <int CTRL> __device__ __forceinline__ float dpp(float x) { return uf((unsigned)__builtin_amdgcn_update_dpp(0, (int)fu(x), CTRL, 0xf, 0xf, true)); }
template <int CTRL> __device__ __forceinline__ unsigned dppu(unsigned x) { return (unsigned)__builtin_amdgcn_update_dpp(0, (int)x, CTRL, 0xf, 0xf, true); }
constexpr int ROR1 = 0x121, ROR2 = 0x122, ROR4 = 0x124, ROR8 = 0x128, XOR2 = 0x4E;
__device__ __forceinline__ float fmx(float a, float b) { return __builtin_elementwise_maximum(a, b); }
__device__ __forceinline__ float row_max(float v) {
  float r;
  asm volatile("s_nop 1\n\tv_max_f32_dpp %0, %1, %1 row_ror:8 row_mask:0xf bank_mask:0xf\n\ts_nop 1\n\tv_max_f32_dpp %0, %0, %0 row_ror:4 row_mask:0xf bank_mask:0xf\n\t"
               "s_nop 1\n\tv_max_f32_dpp %0, %0, %0 row_ror:2 row_mask:0xf bank_mask:0xf\n\ts_nop 1\n\tv_max_f32_dpp %0, %0, %0 row_ror:1 row_mask:0xf bank_mask:0xf"
               : "=&v"(r) : "v"(v));
  return r;
}
__device__ __forceinline__ float tsum32(float a, float b) { auto t = __builtin_amdgcn_permlane32_swap(fu(a), fu(b), false, false); return uf(t[0]) + uf(t[1]); }
__device__ __forceinline__ float tsum16(float a, float b) { auto t = __builtin_amdgcn_permlane16_swap(fu(a), fu(b), false, false); return uf(t[0]) + uf(t[1]); }
__device__ __forceinline__ float xrow_sum(float x) { x = tsum16(x, x); return tsum32(x, x); }
__device__ __forceinline__ float xrow_max(float x) {
  auto s = __builtin_amdgcn_permlane16_swap(fu(x), fu(x), false, false); x = fmx(uf(s[0]), uf(s[1]));
  auto t = __builtin_amdgcn_permlane32_swap(fu(x), fu(x), false, false); return fmx(uf(t[0]), uf(t[1]));
}
__device__ __forceinline__ f32x4 mfma32(v4u a, v4u b, f32x4 c) {
  return __builtin_amdgcn_mfma_f32_16x16x32_bf16(__builtin_bit_cast(b16x8, a), __builtin_bit_cast(b16x8, b), c, 0, 0, 0);
}
#define LP(T, a) ((LAS T*)(size_t)(unsigned)(a))
__device__ __forceinline__ v2u tr_read(int a) { return __builtin_bit_cast(v2u, __builtin_amdgcn_ds_read_tr16_b64_v4i16(LP(s16x4, a))); }
__device__ __forceinline__ v4u cat(v2u a, v2u b) { return v4u{a.x, a.y, b.x, b.y}; }
__device__ __forceinline__ v4u lds128(int a) { return *LP(v4u, a); }
__device__ __forceinline__ f32x4 ldsf4(int a) { return *LP(f32x4, a); }
__device__ __forceinline__ v4u ldg128(const void* base, int voff) {
  v4u r; asm volatile("global_load_dwordx4 %0, %1, %2" : "=v"(r) : "v"(voff), "s"(base) : "memory"); return r;
}
__device__ __forceinline__ unsigned ldg16(const void* base, int voff) {
  unsigned r; asm volatile("global_load_ushort %0, %1, %2 nt" : "=v"(r) : "v"(voff), "s"(base) : "memory"); return r;
}
__device__ __forceinline__ void dma16(const void* base, int voff, unsigned lds_dst) {
  asm volatile("s_mov_b32 m0, %2\n\ts_nop 0\n\tglobal_load_lds_dwordx4 %0, %1 nt" ::"v"(voff), "s"(base), "s"(lds_dst) : "memory");
}
#define WAIT_VM(n) asm volatile("s_waitcnt vmcnt(" #n ")" ::: "memory")
#define BAR() asm volatile("s_waitcnt lgkmcnt(0)\n\ts_barrier" ::: "memory")
#define SCHED() __builtin_amdgcn_sched_barrier(0)
#define PIN(x) asm volatile("" : "+v"(x))
template <int N> __device__ __forceinline__ void nap() {
  if (N > 0) __builtin_amdgcn_s_sleep(N > 127 ? 127 : N);
}
__device__ __forceinline__ int swz(int row) { return ((row & 3) << 2) | ((row >> 2) & 3); }

__device__ __forceinline__ void put(__amdgpu_buffer_rsrc_t rs, int off, unsigned a, unsigned b, unsigned tag) {
  v4u v = {a, tag, b, tag};
  __builtin_amdgcn_raw_buffer_store_b128(v, rs, off, 0, 16);
}

__device__ __forceinline__ int gather1(__amdgpu_buffer_rsrc_t rs, int o0, unsigned tag, v4u& v0, unsigned long long mask = ~0ull) {
  unsigned long long t; int cnt = SPIN_CAP;
  asm volatile("s_mov_b64 exec, %[m]\n"
               "1:\n\tbuffer_load_dwordx4 v[96:99], %[o0], %[rs], 0 offen sc1\n\ts_waitcnt vmcnt(0)\n\t"
               "v_cmp_ne_u32 vcc, %[tag], v97\n\tv_cmp_ne_u32 %[t], %[tag], v99\n\ts_or_b64 %[t], %[t], vcc\n\ts_cbranch_scc0 2f\n\t"
               "s_mov_b64 exec, %[t]\n\ts_sub_u32 %[c], %[c], 1\n\ts_cbranch_scc0 1b\n"
               "2:\n\ts_mov_b64 exec, -1"
               : "+{v[96:99]}"(v0), [t] "=&s"(t), [c] "+&s"(cnt) : [o0] "v"(o0), [rs] "s"(rs), [tag] "s"(tag), [m] "s"(mask) : "vcc", "scc", "memory");
  return SPIN_CAP - cnt + 1;
}
__device__ __forceinline__ int gather2(__amdgpu_buffer_rsrc_t rs, int o0, int o1, unsigned tag, v4u& v0, v4u& v1) {
  unsigned long long t, u; int cnt = SPIN_CAP;
  asm volatile("1:\n\tbuffer_load_dwordx4 v[96:99], %[o0], %[rs], 0 offen sc1\n\tbuffer_load_dwordx4 v[100:103], %[o1], %[rs], 0 offen sc1\n\ts_waitcnt vmcnt(1)\n\t"
               "v_cmp_ne_u32 vcc, %[tag], v97\n\tv_cmp_ne_u32 %[t], %[tag], v99\n\ts_or_b64 %[u], %[t], vcc\n\ts_waitcnt vmcnt(0)\n\t"
               "v_cmp_ne_u32 vcc, %[tag], v101\n\tv_cmp_ne_u32 %[t], %[tag], v103\n\ts_or_b64 %[u], %[u], vcc\n\ts_or_b64 %[t], %[t], %[u]\n\ts_cbranch_scc0 2f\n\t"
               "s_mov_b64 exec, %[t]\n\ts_sub_u32 %[c], %[c], 1\n\ts_cbranch_scc0 1b\n"
               "2:\n\ts_mov_b64 exec, -1"
               : "+{v[96:99]}"(v0), "+{v[100:103]}"(v1), [t] "=&s"(t), [u] "=&s"(u), [c] "+&s"(cnt)
               : [o0] "v"(o0), [o1] "v"(o1), [rs] "s"(rs), [tag] "s"(tag) : "vcc", "scc", "memory");
  return SPIN_CAP - cnt + 1;
}
__device__ __forceinline__ int gather4(__amdgpu_buffer_rsrc_t rs, int o0, unsigned tag, v4u (&v)[4]) {
  unsigned long long t, u; int cnt = SPIN_CAP;
  asm volatile("1:\n\tbuffer_load_dwordx4 v[96:99], %[o0], %[rs], 0 offen sc1\n\tbuffer_load_dwordx4 v[100:103], %[o0], %[rs], %[s1] offen sc1\n\t"
               "buffer_load_dwordx4 v[104:107], %[o0], %[rs], %[s2] offen sc1\n\tbuffer_load_dwordx4 v[108:111], %[o0], %[rs], %[s3] offen sc1\n\ts_waitcnt vmcnt(3)\n\t"
               "v_cmp_ne_u32 vcc, %[tag], v97\n\tv_cmp_ne_u32 %[t], %[tag], v99\n\ts_or_b64 %[u], %[t], vcc\n\ts_waitcnt vmcnt(2)\n\t"
               "v_cmp_ne_u32 vcc, %[tag], v101\n\tv_cmp_ne_u32 %[t], %[tag], v103\n\ts_or_b64 %[u], %[u], vcc\n\ts_or_b64 %[u], %[t], %[u]\n\ts_waitcnt vmcnt(1)\n\t"
               "v_cmp_ne_u32 vcc, %[tag], v105\n\tv_cmp_ne_u32 %[t], %[tag], v107\n\ts_or_b64 %[u], %[u], vcc\n\ts_or_b64 %[u], %[t], %[u]\n\ts_waitcnt vmcnt(0)\n\t"
               "v_cmp_ne_u32 vcc, %[tag], v109\n\tv_cmp_ne_u32 %[t], %[tag], v111\n\ts_or_b64 %[u], %[u], vcc\n\ts_or_b64 %[t], %[t], %[u]\n\ts_cbranch_scc0 2f\n\t"
               "s_mov_b64 exec, %[t]\n\ts_sub_u32 %[c], %[c], 1\n\ts_cbranch_scc0 1b\n"
               "2:\n\ts_mov_b64 exec, -1"
               : "+{v[96:99]}"(v[0]), "+{v[100:103]}"(v[1]), "+{v[104:107]}"(v[2]), "+{v[108:111]}"(v[3]), [t] "=&s"(t), [u] "=&s"(u), [c] "+&s"(cnt)
               : [o0] "v"(o0), [rs] "s"(rs), [tag] "s"(tag), [s1] "s"(4096), [s2] "s"(8192), [s3] "s"(12288) : "vcc", "scc", "memory");
  return SPIN_CAP - cnt + 1;
}

__global__ void __launch_bounds__(NT, 2) fused_block(Params p) {
  extern __shared__ __attribute__((aligned(16))) unsigned char smem[];
  const int LB = (int)(unsigned)(size_t)(LAS unsigned char*)smem;
  const int tid = threadIdx.x, lane = tid & 63, wave = __builtin_amdgcn_readfirstlane(tid >> 6);
  const int bid = blockIdx.x, g = bid & 7, j = bid >> 3, b = j >> 4, s = j & 15;
  const int q = lane >> 4, n = lane & 15, par = lane & 1;
  asm volatile("" ::"s"(p.b1), "s"(p.b2), "s"(p.out), "s"(p.x));
  const int kq = j >> 3, c64 = j & 7;
  const __amdgpu_buffer_rsrc_t rs = __builtin_amdgcn_make_buffer_rsrc((void*)p.ws, 0, WS_END, 0x00020000);
  const int vi = j * 4 + g;
  const bool voter = g < 4 && j < NVJ && wave == 1;
  const bool vpx = j < NVJ / 2;
  const int po = (vpx ? WS_PB + 384 : WS_PD + PD_PR) + (vi & (2 * NVJ - 1)) * 16384 + (j & 1) * 8192, po2 = vpx ? 1024 : 256;
  if (voter) put(rs, lane < 24 ? po + (lane < 16 ? (lane >> 3) * 512 : po2) + (lane & 7) * 16 : OOB, 0u, 0u, 0u);
  unsigned er;
  asm volatile("s_load_dword %0, %1, 0x0" : "=s"(er) : "s"(p.ws) : "memory");
  v4u xs0 = {0u, 0u, 0u, 0u}, xs1 = xs0; v2u xp = {0u, 0u};
  if (wave == 0) {
    xs0 = ldg128(p.x, par * 1024 + (lane >> 1) * 32); xs1 = ldg128(p.x, par * 1024 + (lane >> 1) * 32 + 16);
    asm volatile("global_load_dwordx2 %0, %1, %2" : "=v"(xp) : "v"(par * 1024 + kq * 256 + (lane >> 1) * 8), "s"(p.x) : "memory");
  }
#pragma unroll
  for (int i = 0; i < 2; ++i) {
    const int r = 16 * wave + 8 * i + (lane >> 3);
    dma16(p.wq, (128 * kq + r) * 8192 + g * 1024 + c64 * 128 + (((lane & 7) ^ ((((r >> 1) & 1) | ((r >> 2) & 2)) << 1)) << 4), LB + L_WQ + wave * 2048 + i * 1024);
  }
  if (voter) {
    unsigned long long t1, t2, t3; unsigned w_;
    asm volatile("s_load_dword %3, %4, 0x200\n\ts_waitcnt lgkmcnt(0)\n\ts_memtime %0\n\ts_waitcnt lgkmcnt(0)\n\ts_load_dword %3, %4, 0x0\n\ts_waitcnt lgkmcnt(0)\n\t"
                 "s_memtime %1\n\ts_waitcnt lgkmcnt(0)"
                 : "=&s"(t1), "=&s"(t2), "=&s"(t3), "=&s"(w_) : "s"(p.ws + po), "s"(po2) : "memory");
    asm volatile("" : "+s"(er));
    const unsigned vote = min((unsigned)t2 - (unsigned)t1, 900u);
    put(rs, lane < 8 ? WS_V + vi * 128 + lane * 16 : OOB, vote, vote, er + 1u);
  }
  const size_t kvoff = ((size_t)(b * 8 + g) * SK + (SK - WIN)) * 256 + s * 1024;
  const unsigned char* kb = (const unsigned char*)p.kc + kvoff;
  const unsigned char* vb = (const unsigned char*)p.vc + kvoff;
  int ko[4];
#pragma unroll
  for (int i = 0; i < 4; ++i) { ko[i] = (i * 8 + wave) * 16384 + q * 256 + ((n ^ (4 * i + q)) << 4); PIN(ko[i]); }
#define KLD(i) dma16(kb, ko[i], LB + L_K + wave * 4096 + (i) * 1024)
  if (wave >= 4) { nap<DLY_K>(); KLD(0); KLD(1); KLD(2); KLD(3); }
  int off_q = (q < 2 && n < 8) ? WS_QX + (((g * 2 + q) * 4 + kq) * 32 + c64 * 4 + wave) * 128 + n * 16 : OOB;
  int a_xa = LB + L_XN + (n & 1) * XS + q * 16;
  int a_wq = LB + L_WQ + (8 * q + (n >> 2)) * 128 + (((2 * wave + ((n >> 1) & 1)) ^ ((((n >> 3) & 1) | ((q & 1) << 1)) << 1)) << 4) + (n & 1) * 8;
  PIN(off_q); PIN(a_xa); PIN(a_wq);
  if (wave == 0) {
    asm volatile("s_waitcnt vmcnt(2)" : "+v"(xs0), "+v"(xs1), "+v"(xp)::"memory");
    float s1 = 0.f, s2 = 0.f;
#pragma unroll
    for (int i = 0; i < 4; ++i) {
      s1 = dot2(xs0[i], 0x3F803F80u, s1); s2 = dot2(xs0[i], xs0[i], s2);
      s1 = dot2(xs1[i], 0x3F803F80u, s1); s2 = dot2(xs1[i], xs1[i], s2);
    }
    s1 += dpp<XOR2>(s1); s2 += dpp<XOR2>(s2);
    s1 += dpp<ROR4>(s1); s2 += dpp<ROR4>(s2);
    s1 += dpp<ROR8>(s1); s2 += dpp<ROR8>(s2);
    s1 = xrow_sum(s1); s2 = xrow_sum(s2);
    const float mean = s1 * (1.f / DM);
    const float var = fmaxf(fmaf(-mean, mean, s2 * (1.f / DM)), 0.f);
    const float mb = rbf(mean);
    const float rden = __builtin_amdgcn_rcpf(rbf(__builtin_amdgcn_sqrtf(rbf(rbf(var) + 1e-5f))));
    const unsigned d0 = pk2(bflo(xp.x) - mb, bfhi(xp.x) - mb), d1 = pk2(bflo(xp.y) - mb, bfhi(xp.y) - mb);
    *LP(v2u, LB + L_XN + par * XS + (lane >> 1) * 8) = v2u{pk2(bflo(d0) * rden, bfhi(d0) * rden), pk2(bflo(d1) * rden, bfhi(d1) * rden)};
  }
  if (wave >= 4) WAIT_VM(4); else if (voter) WAIT_VM(1); else WAIT_VM(0);
  asm volatile("s_waitcnt lgkmcnt(0)" : "+s"(er)::"memory");
  const unsigned tag = er + 1u;
  const int vo = WS_V + (lane & (4 * NVJ - 1)) * 128;
  v4u vv = {0u, 0u, 0u, 0u};
  *LP(unsigned, LB + L_S + 12) = er;
  BAR();
  if (wave < 4) {
    {
      v4u a[4]; v2u bw[4][2];
#pragma unroll
      for (int i = 0; i < 4; ++i) { a[i] = lds128(a_xa + i * 64); bw[i][0] = tr_read(a_wq + i * 4096); bw[i][1] = tr_read(a_wq + i * 4096 + 512); }
      SCHED();
      KLD(0); KLD(1); KLD(2); KLD(3);
      SCHED();
      f32x4 acc = {0.f, 0.f, 0.f, 0.f};
#pragma unroll
      for (int i = 0; i < 4; ++i) acc = mfma32(a[i], cat(bw[i][0], bw[i][1]), acc);
      const float v = q ? acc[1] : acc[0];
      put(rs, off_q, fu(v), fu(dpp<ROR8>(v)), tag);
    }
  }
  SCHED();
  const bool is_wo = j < 16, is_mlp = (j >= 16) && (j < 30), is_red = j < 8;
  const int d16 = j >> 1, ch = j & 1;
  const int mi = g * 14 + (is_mlp ? j - 16 : 0), fb = mi >> 1, mh = mi & 1, ri = g * 8 + j;
  int off_pb = WS_PB + ((g * 8 + wave) * 2 + b) * 16384 + s * 512 + (n & 4) * 64 + q * 64 + (n & 3) * 16;
  const bool pb_on = !(n & 4) || q < 2;
  const int off_pw = pb_on ? off_pb + ((n & 8) << 10) : OOB;
  off_pb = (pb_on && n < 8) ? off_pb + ((g & 4) << 11) : OOB;
  const int col_c = is_wo ? ch * 256 + q * 128 + 16 * wave + n : mh * 256 + 32 * wave + lane;
  int off_p2 = (is_wo ? WS_PC + (g * 8 + d16) * 128 : WS_PD + fb * 128) + ((col_c >> 3) & 31) * 16384 + ((col_c & 256) << 5) + (col_c & 7) * 16;
  off_p2 = (lane < 32 && j < 30) ? off_p2 : OOB;
  int tr0 = LB + (8 * q + (n >> 2)) * 256 + (n & 1) * 8 + (((2 * wave + ((n >> 1) & 1)) ^ swz(8 * q + (n >> 2))) << 4);
  int tr1 = LB + (8 * q + (n >> 2)) * 256 + (n & 1) * 8 + (((2 * wave + ((n >> 1) & 1)) ^ swz(8 * q + 4 + (n >> 2))) << 4) + 1024;
  const int dA = is_wo ? LB + L_W + wave * 4096 : LB + L_WQ + wave * 2048, dB = is_wo ? dA + 2048 : dA;
  int wo0, wo1, wo2, wo3;
  if (is_wo) {
    wo0 = ((g * 4 + (wave & 3)) * 128 + 16 * d16 + q) * 1024 + ch * 512 + (wave >> 2) * 256;
    wo1 = wo0 + 4096 + ((n ^ swz(4 + q)) << 4); wo2 = wo0 + 8192 + ((n ^ swz(8 + q)) << 4); wo3 = wo0 + 12288 + ((n ^ swz(12 + q)) << 4);
    wo0 += (n ^ swz(q)) << 4;
  } else {
    const int r2 = 4 * wave + (lane >> 5);
    wo0 = (fb * 32 + r2) * 1024 + mh * 512;
    wo1 = wo0 + 2048 + (((lane & 31) ^ ((((r2 + 2) & 3) | ((r2 >> 3) & 1) << 2) << 1)) << 4);
    wo0 += ((lane & 31) ^ (((r2 & 3) | ((r2 >> 3) & 1) << 2) << 1)) << 4;
    wo2 = wo0; wo3 = wo1;
  }
  int a_q = LB + L_Q + (n >> 2) * QS + q * 16, a_p = LB + L_P + (n & 3) * QS + q * 16, a_pw = LB + L_P + q * QS + (16 * wave + n) * 2;
  int a_mx = LB + L_MAX + q * 32, a_mw = a_mx + wave * 4, a_m = LB + L_M + (n & 3) * 4, a_mq = LB + L_M + q * 4;
  int a_k = LB + L_K + wave * 4096 + n * 256;
  int qw = LB + L_Q + wave * QS + (lane >> 3) * 32 + (lane & 7) * 2, qg = WS_QX + (g * 2 + b) * 16384 + tid * 16;
  PIN(off_pb); PIN(off_p2); PIN(tr0); PIN(tr1); PIN(wo0); PIN(wo1); PIN(wo2); PIN(wo3);
  PIN(a_q); PIN(a_p); PIN(a_pw); PIN(a_mx); PIN(a_mw); PIN(a_m); PIN(a_mq); PIN(a_k); PIN(qw); PIN(qg);
  *LP(unsigned, LB + L_XN + 2 * XS + (tid & 255) * 4) = 0x3F803F80u;
  *LP(unsigned, LB + L_Z + (lane & 3) * 4) = 0u;
  if (wave < 4) {
    nap<DLY_A>();
    *LP(volatile unsigned, LB + L_S + 12) = tag;
    {
      v4u v[4] = {};
      gather4(rs, qg, tag, v);
      const unsigned pk = pk2((uf(v[0].x) + uf(v[1].x)) + (uf(v[2].x) + uf(v[3].x)), (uf(v[0].z) + uf(v[1].z)) + (uf(v[2].z) + uf(v[3].z)));
      *LP(u16, qw) = (u16)pk;
      *LP(u16, qw + 16) = (u16)(pk >> 16);
    }
  } else {
    const int voff = (wave - 4) * 32768 + q * 256;
    const unsigned vdst = LB + L_V + (wave - 4) * 8192;
#define VLD(t) dma16(vb, voff + (((t) >> 1) * 8 + ((t) & 1)) * 16384 + ((n ^ swz(4 * ((t) >> 1) + q)) << 4), vdst + ((t) & 1) * 4096 + ((t) >> 1) * 1024)
    int pw0 = off_pw, pw1 = pb_on ? off_pw - 4 * 32768 : OOB;
    PIN(pw0); PIN(pw1);
    WAIT_VM(1);
    nap<DLY_V>();
    VLD(0); VLD(1);
    VLD(2); VLD(3);
#pragma nounroll
    for (int i = 0; i < 48; ++i) {
      if (*LP(volatile unsigned, LB + L_S + 12) == tag) break;
      __builtin_amdgcn_s_sleep(1);
    }
    nap<DLY_H>();
    VLD(4);
    VLD(5); VLD(6);
    put(rs, pw0, 0u, 0u, tag - 1u); put(rs, pw1, 0u, 0u, tag - 1u);
    asm volatile("buffer_load_dwordx4 %0, %1, %2, 0 offen sc1" : "+v"(vv) : "v"(wave == 4 ? vo : OOB), "s"(rs) : "memory");
    VLD(7);
    WAIT_VM(11);
#undef VLD
  }
  v4u kf[4];
#pragma unroll
  for (int i = 0; i < 4; ++i) kf[i] = lds128(a_k + (((4 * i + q) ^ n) << 4));
  BAR();
  float sc;
  int pol, polx;
  {
    v4u qa[4];
#pragma unroll
    for (int i = 0; i < 4; ++i) qa[i] = lds128(a_q + i * 64);
    SCHED();
    f32x4 acc = {0.f, 0.f, 0.f, 0.f};
#pragma unroll
    for (int i = 0; i < 4; ++i) acc = mfma32(qa[i], kf[i], acc);
    SCHED();
    if (is_wo) { dma16(p.wo, wo0, dA); dma16(p.wo, wo1, dA + 1024); }
    SCHED();
    sc = rbf(rbf(acc[0]) * 0.08838834764831845f);
    *LP(float, a_mw) = row_max(sc);
  }
  BAR();
  v2u vf[4][2];
  {
    const f32x4 m0 = ldsf4(a_mx), m1 = ldsf4(a_mx + 16);
    SCHED();
    if (is_wo) { dma16(p.wo, wo2, dB); dma16(p.wo, wo3, dB + 1024); }
    SCHED();
    const float M = fmx(fmx(fmx(fmx(m0[0], m0[1]), m0[2]), fmx(fmx(m0[3], m1[0]), m1[1])), fmx(m1[2], m1[3]));
    const float pr = __builtin_amdgcn_exp2f((sc - M) * LOG2E);
    *LP(u16, a_pw) = (u16)pk2(pr, pr);
    *LP(float, a_mq) = M;
  }
  if (wave == 4) {
    if (is_wo) WAIT_VM(5); else WAIT_VM(1);
    PIN(vv);
    if (__builtin_amdgcn_ballot_w64((vv.y != tag) | (vv.w != tag)) != 0ull) gather1(rs, vo, tag, vv);
    int dv = (n & 4) ? -(int)vv.x : (int)vv.x;
    dv += (int)dppu<ROR8>((unsigned)dv); dv += (int)dppu<ROR4>((unsigned)dv); dv += (int)dppu<ROR2>((unsigned)dv); dv += (int)dppu<ROR1>((unsigned)dv);
    { auto t = __builtin_amdgcn_permlane16_swap((unsigned)dv, (unsigned)dv, false, false); dv = (int)t[0] + (int)t[1]; }
    *LP(int, LB + L_S + (q >> 1) * 4) = dv > 0 ? 8192 : 0;
  }
  if (is_wo) WAIT_VM(4); else WAIT_VM(0);
  PIN(vv);
  BAR();
  {
    v4u pa[4];
#pragma unroll
    for (int i = 0; i < 4; ++i) {
      pa[i] = lds128(a_p + i * 64);
      vf[i][0] = tr_read(tr0 + L_V + i * 8192); vf[i][1] = tr_read(tr1 + L_V + i * 8192);
    }
    float Mh = *LP(float, a_m);
    { const v2u pp = *LP(v2u, LB + L_S); polx = (int)pp.x; pol = (int)pp.y; }
    SCHED();
    f32x4 o = {0.f, 0.f, 0.f, 0.f};
    float l0 = 0.f, l1 = 0.f;
#pragma unroll
    for (int i = 0; i < 4; ++i) {
      o = mfma32(cat(vf[i][0], vf[i][1]), pa[i], o);
      l0 = dot2(pa[i].x, 0x3F803F80u, l0); l1 = dot2(pa[i].y, 0x3F803F80u, l1);
      l0 = dot2(pa[i].z, 0x3F803F80u, l0); l1 = dot2(pa[i].w, 0x3F803F80u, l1);
    }
    off_pb ^= polx; off_p2 ^= pol;
    float Ls = xrow_sum(l0 + l1);
    PIN(Ls); PIN(Mh);
    unsigned o01 = pk2(o[0], o[1]), o23 = pk2(o[2], o[3]);
    PIN(o01); PIN(o23);
    put(rs, off_pb, n < 4 ? o01 : fu(Mh), n < 4 ? o23 : fu(Ls), tag);
  }
  if (is_wo) {
    unsigned sm0 = 0, sm1 = 0;
    if (is_red) { const int c = ri * 8 + (lane & 7); sm0 = ldg16(p.x, ((lane >> 5) * DM + c) * 2); sm1 = ldg16(p.b2, c * 2); }
    const int bb = wave >> 2, dqd = wave & 3;
    const int mine = ((g & 4) << 11) ^ pol;
    int base = WS_PB + ((g * 8 + d16) * 2 + bb) * 16384 + (((g & 4) << 11) ^ polx) + (lane >> 2) * 512 + (lane & 3) * 16;
    int lo_ = LB + L_O + bb * 128 + ((lane & 3) * 16 + dqd * 4 + q) * 2;
    int base0 = base + dqd * 64;
    base += 256;
    int a_o = LB + L_O + (n & 1) * 128 + q * 16;
    int tw0 = tr0 + L_W, tw1 = tr1 + L_W;
    PIN(base); PIN(base0); PIN(lo_); PIN(a_o); PIN(tw0); PIN(tw1);
    nap<DLY_B>();
    {
      v4u v[2] = {};
      gather2(rs, base0, base, tag, v[0], v[1]);
      PIN(sm0); PIN(sm1);
      const float m = uf(v[1].x);
      float M = fmx(m, dpp<ROR4>(m)); M = fmx(M, dpp<ROR8>(M)); M = xrow_max(M);
      const float w = __builtin_amdgcn_exp2f((m - M) * LOG2E);
      float Ls = w * uf(v[1].z);
      Ls += dpp<ROR4>(Ls); Ls += dpp<ROR8>(Ls); Ls = xrow_sum(Ls);
      const float e0 = tsum32(w * bflo(v[0].x), w * bflo(v[0].z));
      const float e1 = tsum32(w * bfhi(v[0].x), w * bfhi(v[0].z));
      float e = tsum16(e0, e1);
      e += dpp<ROR4>(e); e += dpp<ROR8>(e);
      const float o = e * __builtin_amdgcn_rcpf(Ls);
      *LP(u16, lo_) = (u16)pk2(o, o);
    }
    BAR();
    {
      const v4u oa0 = lds128(a_o), oa1 = lds128(a_o + 64);
      v2u w[2][2][2];
#pragma unroll
      for (int hh = 0; hh < 2; ++hh)
#pragma unroll
        for (int i = 0; i < 2; ++i) { w[hh][i][0] = tr_read(tw0 + hh * 16384 + i * 8192); w[hh][i][1] = tr_read(tw1 + hh * 16384 + i * 8192); }
      SCHED();
      f32x4 y0 = mfma32(oa0, cat(w[0][0][0], w[0][0][1]), f32x4{0.f, 0.f, 0.f, 0.f});
      f32x4 y1 = mfma32(oa0, cat(w[1][0][0], w[1][0][1]), f32x4{0.f, 0.f, 0.f, 0.f});
      y0 = mfma32(oa1, cat(w[0][1][0], w[0][1][1]), y0);
      y1 = mfma32(oa1, cat(w[1][1][0], w[1][1][1]), y1);
      put(rs, off_p2, fu(q ? y1[0] : y0[0]), fu(q ? y1[1] : y0[1]), tag);
    }
    if (is_red) {
      int oc = WS_PC + (ri & 31) * 16384 + mine + wave * 1024 + lane * 16;
      int od = WS_PD + (ri & 31) * 16384 + mine + wave * (7 * 128) + lane * 16;
      int rr = LB + L_RED + ((lane >> 5) * 8 + (lane & 7)) * 32, rw = rr + wave * 4;
      int oh = lane < 16 ? WS_PD + (ri & 31) * 16384 + PD_HX + (ri >> 5) * 128 + ((lane & 8) << 10) + (lane & 7) * 16 : OOB;
      int oo = ((lane >> 5) * DM + ri * 8 + (lane & 7)) * 2;
      float xr = bflo(sm0), b2v = bflo(sm1);
      PIN(oc); PIN(od); PIN(rw); PIN(rr); PIN(oh); PIN(oo); PIN(xr); PIN(b2v);
      nap<DLY_C1>();
      float h = 0.f;
      {
        v4u v = {};
        gather1(rs, oc, tag, v);
        float y = tsum32(uf(v.x), uf(v.z));
        y = tsum16(y, y);
        y += dpp<ROR8>(y);
        *LP(float, rw) = y;
      }
      BAR();
      if (wave == 0) {
        const f32x4 r0 = ldsf4(rr), r1 = ldsf4(rr + 16);
        const f32x4 t4 = r0 + r1;
        h = rbf(xr + rbf((t4[0] + t4[1]) + (t4[2] + t4[3])));
        auto t = __builtin_amdgcn_permlane32_swap(fu(h), fu(h), false, false);
        const unsigned pk = (t[0] >> 16) | (t[1] & 0xffff0000u);
        put(rs, oh, pk, dppu<ROR4>(pk), tag);
      }
      nap<DLY_D>();
      {
        v4u v = {};
        gather1(rs, od, tag, v, (1ull << 56) - 1);
        float y = tsum32(uf(v.x), uf(v.z));
        y = tsum16(y, y);
        y += dpp<ROR8>(y);
        *LP(float, rw + 512) = y;
      }
      BAR();
      if (wave == 0) {
        const f32x4 r0 = ldsf4(rr + 512), r1 = ldsf4(rr + 528);
        const f32x4 t4 = r0 + r1;
        const float r = h + rbf(rbf((t4[0] + t4[1]) + (t4[2] + t4[3])) + b2v);
        *(u16*)((unsigned char*)p.out + oo) = (u16)pk2(r, r);
        if (ri == 0 && lane == 0) __builtin_amdgcn_raw_buffer_store_b32(tag, rs, WS_E, 0, 16);
      }
    }
  } else if (is_mlp) {
    nap<DLY_M>();
    {
      const int rot = mi & 3;
      const int w1o = (64 * wave + (lane >> 2)) * (FF * 2) + fb * 64 + (lane & 3) * 16;
#pragma unroll
      for (int i = 0; i < 4; ++i) {
        const int ii = (i + rot) & 3;
        dma16(p.w1, w1o + ii * (16 * FF * 2), LB + L_W + wave * 4096 + ii * 1024);
        if (i == 0) { dma16(p.w2, wo0, dA); dma16(p.w2, wo1, dA + 1024); }
      }
    }
    unsigned sm0 = ldg16(p.b1, (fb * 32 + (lane & 31)) * 2);
    int hoff = WS_PD + PD_HX + (((g & 4) << 11) ^ pol) + (8 * (wave & 3) + ((lane >> 2) & 7)) * 16384 + (wave >> 2) * 128 + (lane & 3) * 16;
    int xw = LB + L_XN + (lane >> 5) * XS + wave * 128 + (lane & 31) * 4;
    int aoff = LB + L_XN + ((n & 3) < 2 ? (n & 3) : 2) * XS + wave * 128 + q * 16;
    int rw = LB + L_V + wave * 512 + (lane & 31) * 16, rr = LB + L_V + (lane >> 5) * 2048 + (lane & 31) * 16;
    int gw = LB + L_G + wave * 128 + lane * 2;
    int ga0 = (n >> 2) == 0 ? LB + L_G + wave * 128 + (n & 1) * 64 + q * 16 : LB + L_Z;
    int ga1 = (n >> 2) == 1 ? LB + L_G + wave * 128 + (n & 1) * 64 + q * 16 : LB + L_Z;
    unsigned psel = lane < 32 ? 0x05040100u : 0x07060302u;
    PIN(hoff); PIN(xw); PIN(aoff); PIN(rw); PIN(rr); PIN(gw); PIN(ga0); PIN(ga1); PIN(psel);
    const int e_ = n >> 2;
    int boff = LB + L_W + wave * 4096 + (8 * q + (e_ >> 1) + 4 * (e_ & 1)) * 64 + (n & 3) * 8;
    const int f2 = ((n >> 2) | (q & 1) << 2) << 1;
    int wa0 = LB + L_WQ + (8 * q + (n >> 2)) * 512 + (((4 * wave + ((n >> 1) & 1)) ^ f2) << 4) + (n & 1) * 8;
    int wa1 = LB + L_WQ + (8 * q + (n >> 2)) * 512 + (((4 * wave + 2 + ((n >> 1) & 1)) ^ f2) << 4) + (n & 1) * 8;
    PIN(boff); PIN(wa0); PIN(wa1);
    asm volatile("s_waitcnt vmcnt(0)" : "+v"(sm0)::"memory");
    v4u bw[2][2], wf[2];
#pragma unroll
    for (int tt = 0; tt < 2; ++tt) {
#pragma unroll
      for (int i = 0; i < 2; ++i) { bw[tt][i] = cat(tr_read(boff + tt * 32 + i * 2048), tr_read(boff + tt * 32 + i * 2048 + 128)); }
    }
    float bias = bflo(sm0);
    PIN(bias);
    {
      v4u v = {};
      gather1(rs, hoff, tag, v);
      *LP(unsigned, xw) = __builtin_amdgcn_perm(v.z, v.x, psel);
    }
    {
      const v4u a0 = lds128(aoff), a1 = lds128(aoff + 64);
      SCHED();
      f32x4 u0 = mfma32(a0, bw[0][0], f32x4{0.f, 0.f, 0.f, 0.f});
      f32x4 u1 = mfma32(a0, bw[1][0], f32x4{0.f, 0.f, 0.f, 0.f});
      f32x4 G = mfma32(a0, a0, f32x4{0.f, 0.f, 0.f, 0.f});
      u0 = mfma32(a1, bw[0][1], u0);
      u1 = mfma32(a1, bw[1][1], u1);
      G = mfma32(a1, a1, G);
      const bool t1 = q & 1;
      *LP(f32x4, rw) = f32x4{t1 ? u1[0] : u0[0], t1 ? u1[1] : u0[1], t1 ? u1[2] : u0[2], (n & 1) ? G[1] : G[0]};
    }
    BAR();
    {
      const f32x4 r0 = ldsf4(rr), r1 = ldsf4(rr + 512), r2 = ldsf4(rr + 1024), r3 = ldsf4(rr + 1536);
      SCHED();
      wf[0] = cat(tr_read(wa0), tr_read(wa0 + 2048)); wf[1] = cat(tr_read(wa1), tr_read(wa1 + 2048));
      SCHED();
      const f32x4 t = (r0 + r1) + (r2 + r3);
      const float ss = tsum32(t[3], t[3]);
      const float ub = tsum32(t[0], t[1]);
      const float cs = tsum32(t[2], t[2]);
      const float q0 = uf(__builtin_amdgcn_readlane(fu(ss), 0)), q1 = uf(__builtin_amdgcn_readlane(fu(ss), 1));
      const float m0 = uf(__builtin_amdgcn_readlane(fu(ss), 2)), m1 = uf(__builtin_amdgcn_readlane(fu(ss), 3));
      const bool b1_ = lane >= 32;
      const float mean = (b1_ ? m1 : m0) * (1.f / DM);
      const float var = fmaxf(fmaf(-mean, mean, (b1_ ? q1 : q0) * (1.f / DM)), 0.f);
      const float mb = rbf(mean);
      const float rden = __builtin_amdgcn_rcpf(rbf(__builtin_amdgcn_sqrtf(rbf(rbf(var) + 1e-5f))));
      const float u = rbf(rbf(fmaf(-mb, cs, ub) * rden) + bias);
      const float inner = u * fmaf(u * u, 0.044715f * 0.7978845608028654f * 2.f * LOG2E, 0.7978845608028654f * 2.f * LOG2E);
      const float gv = u - u * __builtin_amdgcn_rcpf(__builtin_amdgcn_exp2f(inner) + 1.f);
      *LP(u16, gw) = (u16)pk2(gv, gv);
    }
    {
      const v4u g0 = lds128(ga0), g1 = lds128(ga1);
      SCHED();
      f32x4 z = mfma32(g0, wf[0], f32x4{0.f, 0.f, 0.f, 0.f});
      z = mfma32(g1, wf[1], z);
      put(rs, off_p2, fu(z[0]), fu(z[1]), tag);
    }
  } else {
    unsigned d_ = __builtin_amdgcn_raw_buffer_load_b32(rs, ((j & 1) ? WS_PD : WS_PC) + ((g & 3) * 8 + wave) * 16384 + (((g & 4) << 11) ^ pol) + lane * 128, 0, 16);
    asm volatile("s_waitcnt vmcnt(0)" : "+v"(d_)::"memory");
  }
}

extern "C" void kernel_launch(void* const* d_in, const int* in_sizes, int n_in, void* d_out, int out_size, void* d_ws, size_t ws_size,
                              hipStream_t stream) {
  Params p;
  p.x = (const u16*)d_in[0]; p.kc = (const u16*)d_in[1]; p.vc = (const u16*)d_in[2]; p.wq = (const u16*)d_in[3]; p.wo = (const u16*)d_in[4];
  p.w1 = (const u16*)d_in[6]; p.b1 = (const u16*)d_in[7]; p.w2 = (const u16*)d_in[8]; p.b2 = (const u16*)d_in[9];
  p.out = (u16*)d_out; p.ws = (unsigned char*)d_ws;
  (void)hipFuncSetAttribute((const void*)fused_block, hipFuncAttributeMaxDynamicSharedMemorySize, L_END);
  hipLaunchKernelGGL(fused_block, dim3(NWG), dim3(NT), L_END, stream, p);
}
```
